```python
import torch

def _softmax(x, axis=-1):
    return torch.softmax(x, dim=axis)
def _gelu(x):
    return torch.nn.functional.gelu(x, approximate="tanh")
def _silu(x):
    return torch.nn.functional.silu(x)
def _sigmoid(x):
    return torch.sigmoid(x)
def _layernorm(x, axis=-1, eps=1e-5):
    m = x.mean(dim=axis, keepdim=True)
    v = x.var(dim=axis, keepdim=True, unbiased=False)
    return (x - m) / torch.sqrt(v + eps)
def _scatter_add(tgt, idx, src, axis=0):
    return tgt.clone().index_add_(axis, idx, src)
def _segment_sum(src, seg, n):
    out = torch.zeros((n,) + src.shape[1:], dtype=src.dtype)
    return out.index_add_(0, seg, src)
def _pad(x, pw):
    flat = tuple(v for pair in reversed(pw) for v in pair)
    return torch.nn.functional.pad(x, flat)

kernel_name = 'concat_heads__sliding_local_dilated__linear_kernel_hedgehog__prompt'

def setup_inputs():

    _w = (lambda a: (lambda t: t.to(torch.bfloat16) if t.is_floating_point() else t)(torch.as_tensor(a)))
    def _A_setup():
        B, S, D = 1, 2048, 640
        return {
            "x": _w((lambda *s: torch.randn(*s, dtype=torch.bfloat16) * 0.2)(B, S, D)),
            "wqkv": _w((lambda *s: torch.randn(*s, dtype=torch.bfloat16) * 0.2)(D, 3 * D)),
            "wo": _w((lambda *s: torch.randn(*s, dtype=torch.bfloat16) * 0.05)(D, D)),
        }
    _A_in = _A_setup()

    def _B_setup():
        B, S, D, H = 1, 2048, 640, 32
        Dh = D // H
        return {
            "x": _w((lambda *s: torch.randn(*s, dtype=torch.bfloat16) * 0.2)(B, S, D)),
            "wqkv": _w((lambda *s: torch.randn(*s, dtype=torch.bfloat16) * 0.8)(D, 3 * D)),
            "w_phi": _w((lambda *s: torch.randn(*s, dtype=torch.bfloat16) * 0.8)(H, Dh, Dh) * (Dh ** -0.5)),
            "wo": _w((lambda *s: torch.randn(*s, dtype=torch.bfloat16) * 0.2)(D, D)),
        }
    _B_in = _B_setup()

    return {
        "A_x": _A_in["x"],
        "A_wqkv": _A_in["wqkv"],
        "A_wo": _A_in["wo"],
        "B_x": _B_in["x"],
        "B_wqkv": _B_in["wqkv"],
        "B_w_phi": _B_in["w_phi"],
        "B_wo": _B_in["wo"],
    }

def reference(A_x, A_wqkv, A_wo, B_x, B_wqkv, B_w_phi, B_wo):
    def _A_ref(x, wqkv, wo):
        B, S, D = x.shape
        H = 32; Dh = D // H
        W, dil = 8, 2
        qkv = (lambda x, *d: x.permute(*d))((x @ wqkv).reshape(B, S, 3, H, Dh), 2, 0, 3, 1, 4)
        q, k, v = qkv[0], qkv[1], qkv[2]
        scale = Dh ** -0.5
        pos = torch.arange(S)
        off = torch.arange(W) * dil
        idx = pos[:, None] - off[None, :]
        mask = idx >= 0
        idx_c = torch.where(mask, idx, 0).reshape(-1)
        kg = k[:, :, idx_c, :].reshape(B, H, S, W, Dh)
        vg = v[:, :, idx_c, :].reshape(B, H, S, W, Dh)
        logits = torch.einsum("bhsd,bhswd->bhsw", q, kg) * scale
        logits = torch.where(mask[None, None, :, :], logits, -1e9)
        attn = _softmax(logits, axis=-1)
        o = torch.einsum("bhsw,bhswd->bhsd", attn, vg)
        return (lambda x, *d: x.permute(*d))(o, 0, 2, 1, 3).reshape(B, S, D) @ wo

    def _B_ref(x, wqkv, w_phi, wo):
        B, S, D = x.shape
        H = 32; Dh = D // H
        qkv = (lambda x, *d: x.permute(*d))((x @ wqkv).reshape(B, S, 3, H, Dh), 2, 0, 3, 1, 4)
        q, k, v = qkv[0], qkv[1], qkv[2]
        pq = torch.einsum("bhsd,hdf->bhsf", q, w_phi)
        pk = torch.einsum("bhsd,hdf->bhsf", k, w_phi)
        phi_q = torch.concatenate([_softmax(pq, axis=-1), _softmax(-pq, axis=-1)], axis=-1)
        phi_k = torch.concatenate([_softmax(pk, axis=-1), _softmax(-pk, axis=-1)], axis=-1)
        kv = torch.einsum("bhsf,bhsd->bhfd", phi_k, v)
        z  = torch.einsum("bhsf->bhf", phi_k)
        num = torch.einsum("bhqf,bhfd->bhqd", phi_q, kv)
        den = torch.einsum("bhqf,bhf->bhq", phi_q, z)[..., None] + 1e-6
        o = num / den
        return (lambda x, *d: x.permute(*d))(o, 0, 2, 1, 3).reshape(B, S, D) @ wo

    a = _A_ref(A_x, A_wqkv, A_wo)
    b = _B_ref(B_x, B_wqkv, B_w_phi, B_wo)
    return torch.concatenate([a, b], axis=-1)
```

```cpp
#include <hip/hip_runtime.h>
#include <hip/hip_bf16.h>
typedef unsigned short u16;
__device__ __forceinline__ float b2f(u16 x){ return __uint_as_float((unsigned)x<<16); }
__device__ __forceinline__ u16 f2b(float f){ unsigned u=__float_as_uint(f); u += 0x7fff + ((u>>16)&1); return u>>16; }
constexpr int S=2048, D=640, H=32, DH=20;
__global__ void gemm(const u16* A, const u16* B, u16* C, int M, int N, int K, int ldc){
  __shared__ float sa[16][17], sb[16][17];
  int tx=threadIdx.x, ty=threadIdx.y; int r=blockIdx.y*16+ty, c=blockIdx.x*16+tx; float acc=0;
  for(int k0=0;k0<K;k0+=16){ sa[ty][tx]=b2f(A[r*K+k0+tx]); sb[ty][tx]=b2f(B[(k0+ty)*N+c]); __syncthreads();
    for(int k=0;k<16;k++) acc+=sa[ty][k]*sb[k][tx]; __syncthreads(); }
  C[r*ldc+c]=f2b(acc);
}
__global__ void attnA(const u16* qkv, u16* o){
  int idx=blockIdx.x*blockDim.x+threadIdx.x; int s=idx/H, h=idx%H;
  float lg[8]; float m=-1e30f;
  #pragma unroll
  for(int w=0;w<8;w++){ int p=s-2*w; float a=0; if(p>=0){ for(int d=0;d<DH;d++) a+=b2f(qkv[s*1920+h*20+d])*b2f(qkv[p*1920+640+h*20+d]); a*=0.22360679775f;} else a=-1e9f; lg[w]=a; m=fmaxf(m,a);}
  float l=0;
  #pragma unroll
  for(int w=0;w<8;w++){ lg[w]=__expf(lg[w]-m); l+=lg[w]; }
  for(int d=0;d<DH;d++){ float a=0;
    #pragma unroll
    for(int w=0;w<8;w++){ int p=s-2*w; if(p<0)p=0; a+=lg[w]*b2f(qkv[p*1920+1280+h*20+d]); }
    o[s*640+h*20+d]=f2b(a/l); }
}
__global__ void phiB(const u16* qkv, const u16* wphi, float* phi){
  int idx=blockIdx.x*blockDim.x+threadIdx.x; int which=idx/(S*H); int s=(idx/H)%S, h=idx%H;
  float* out=phi+(size_t)idx*40; float mx=-1e30f, mn=1e30f;
  for(int f=0;f<20;f++){ float a=0; for(int d=0;d<20;d++) a+=b2f(qkv[s*1920+which*640+h*20+d])*b2f(wphi[h*400+d*20+f]); out[f]=a; mx=fmaxf(mx,a); mn=fminf(mn,a);}
  float sp=0,sn=0;
  for(int f=0;f<20;f++){ float a=out[f]; float e=__expf(a-mx), g=__expf(mn-a); out[f]=e; out[20+f]=g; sp+=e; sn+=g; }
  for(int f=0;f<20;f++){ out[f]/=sp; out[20+f]/=sn; }
}
__global__ void kvB(const u16* qkv, const float* phi, float* kv){
  int h=blockIdx.x; int t=threadIdx.x; if(t>=840) return; int f=t/21, d=t%21; float a=0;
  const float* pk=phi+(size_t)S*H*40;
  for(int s=0;s<S;s++){ float p=pk[((size_t)s*H+h)*40+f]; float v= d<20? b2f(qkv[s*1920+1280+h*20+d]) : 1.f; a+=p*v; }
  kv[h*840+t]=a;
}
__global__ void oB(const float* phi, const float* kv, u16* o){
  int idx=blockIdx.x*blockDim.x+threadIdx.x; int s=idx/H, h=idx%H; const float* p=phi+(size_t)idx*40;
  float den=1e-6f; for(int f=0;f<40;f++) den+=p[f]*kv[h*840+f*21+20];
  for(int d=0;d<20;d++){ float a=0; for(int f=0;f<40;f++) a+=p[f]*kv[h*840+f*21+d]; o[s*640+h*20+d]=f2b(a/den);}
}
extern "C" void kernel_launch(void* const* d_in, const int* in_sizes, int n_in, void* d_out, int out_size, void* d_ws, size_t ws_size, hipStream_t stream) {
  const u16 *Ax=(const u16*)d_in[0], *Aw=(const u16*)d_in[1], *Ao=(const u16*)d_in[2], *Bx=(const u16*)d_in[3], *Bw=(const u16*)d_in[4], *Bp=(const u16*)d_in[5], *Bo=(const u16*)d_in[6];
  char* ws=(char*)d_ws; u16* qa=(u16*)ws; u16* qb=qa+S*1920; u16* oa=qb+S*1920; u16* ob=oa+S*640; float* phi=(float*)(ob+S*640); float* kv=phi+(size_t)2*S*H*40;
  u16* out=(u16*)d_out;
  gemm<<<dim3(120,128),dim3(16,16),0,stream>>>(Ax,Aw,qa,S,1920,640,1920);
  gemm<<<dim3(120,128),dim3(16,16),0,stream>>>(Bx,Bw,qb,S,1920,640,1920);
  attnA<<<S*H/256,256,0,stream>>>(qa,oa);
  phiB<<<2*S*H/256,256,0,stream>>>(qb,Bp,phi);
  kvB<<<H,896,0,stream>>>(qb,phi,kv);
  oB<<<S*H/256,256,0,stream>>>(phi,kv,ob);
  gemm<<<dim3(40,128),dim3(16,16),0,stream>>>(oa,Ao,out,S,640,640,1280);
  gemm<<<dim3(40,128),dim3(16,16),0,stream>>>(ob,Bo,out+640,S,640,640,1280);
}
```

```cpp
#include <hip/hip_runtime.h>
#include <hip/hip_bf16.h>
#include <type_traits>

#define PROFILE 0
#define NROT 3

#if defined(__HIP_DEVICE_COMPILE__)
#pragma clang attribute push(__attribute__((target("no-packed-fp32-ops"))), apply_to = function)
#endif
typedef unsigned short u16;
typedef unsigned long long u64;
typedef unsigned v2u __attribute__((ext_vector_type(2)));
typedef unsigned v4u __attribute__((ext_vector_type(4)));
typedef int v4i __attribute__((ext_vector_type(4)));
typedef float f2 __attribute__((ext_vector_type(2)));
typedef float f4 __attribute__((ext_vector_type(4)));
typedef short s4 __attribute__((ext_vector_type(4)));
typedef __bf16 bf2 __attribute__((ext_vector_type(2)));
typedef __bf16 bf8 __attribute__((ext_vector_type(8)));
#define LAS __attribute__((address_space(3)))
#define GAS __attribute__((address_space(1)))
#define DI __device__ __forceinline__
typedef LAS char* lptr;

constexpr unsigned WS_O = 0;
constexpr unsigned WS_KP = 5242880;
constexpr unsigned WS_KR = WS_KP + 8 * 36 * 8192;
constexpr unsigned WS_F3 = WS_KR + 8 * 9216;
constexpr unsigned WS_E3 = WS_F3 + 256 * 128;
constexpr unsigned WS_F1 = WS_E3 + 256 * 128;
constexpr unsigned WS_F2 = WS_F1 + 32 * 128;
constexpr unsigned WS_E2 = WS_F2 + 32 * 128;
constexpr unsigned WS_END = WS_E2 + 8 * 128;
constexpr unsigned WS_DBG = 16u << 20;
constexpr unsigned OOB = 0x7f000000u;
constexpr u64 TAG_STEP = 0x9E3779B97F4A7C15ull;
constexpr unsigned SPIN_CAP = 1u << 16;

template <int N> using IC = std::integral_constant<int, N>;
template <int I, int N, class F> DI void sfor_(F&& f) {
  if constexpr (I < N) { f(IC<I>{}); sfor_<I + 1, N>(f); }
}
template <int N, class F> DI void sfor(F&& f) { sfor_<0, N>(f); }

DI unsigned f2u(float x) { return __builtin_bit_cast(unsigned, x); }
DI float u2f(unsigned x) { return __builtin_bit_cast(float, x); }
DI unsigned rfl(unsigned x) { return __builtin_amdgcn_readfirstlane(x); }
DI v4i mk_srd(const void* p, unsigned bytes) {
  u64 a = (u64)p;
  v4i r;
  r.x = rfl((unsigned)a); r.y = rfl((unsigned)(a >> 32) & 0xffffu); r.z = bytes; r.w = 0x00020000;
  return r;
}
template <int IMM> DI void dma(v4i rs, unsigned voff, unsigned soff, unsigned sbase) {
  asm volatile("s_add_u32 m0, %3, %4\n\ts_nop 0\n\tbuffer_load_dwordx4 %0, %1, %2 offen lds"
               :: "v"(voff), "s"(rs), "s"(soff), "s"(sbase), "n"(IMM) : "memory", "scc");
}
template <int N> DI void wait_vm() { asm volatile("s_waitcnt vmcnt(%0)" :: "n"(N) : "memory"); }
DI void bar() { asm volatile("s_waitcnt lgkmcnt(0)\n\ts_barrier" ::: "memory"); }
DI void barx() { asm volatile("s_barrier" ::: "memory"); }
#define SB() __builtin_amdgcn_sched_barrier(0)
DI unsigned pk2(float a, float b) { return __builtin_bit_cast(unsigned, __builtin_convertvector((f2){a, b}, bf2)); }
DI v2u pk4(f4 v) { return (v2u){pk2(v[0], v[1]), pk2(v[2], v[3])}; }
DI v4u cat(v2u a, v2u b) { return (v4u){a[0], a[1], b[0], b[1]}; }
DI f4 mma(v4u a, v4u b, f4 c) {
  return __builtin_amdgcn_mfma_f32_16x16x32_bf16(__builtin_bit_cast(bf8, a), __builtin_bit_cast(bf8, b), c, 0, 0, 0);
}
DI f4 mma4(v2u a, v2u b, f4 c) {
  return __builtin_amdgcn_mfma_f32_4x4x4bf16_1k(__builtin_bit_cast(s4, a), __builtin_bit_cast(s4, b), c, 0, 0, 0);
}
DI v2u trd(lptr p) { return __builtin_bit_cast(v2u, __builtin_amdgcn_ds_read_tr16_b64_v4i16((LAS s4*)p)); }
DI lptr opq(lptr p) { unsigned a = (unsigned)(size_t)p; asm volatile("" : "+v"(a)); return (lptr)(size_t)a; }
DI v4u ld16(lptr p) { return *(LAS v4u*)p; }
DI v2u ld8(lptr p) { return *(LAS v2u*)p; }
DI void st16(lptr p, v4u v) { *(LAS v4u*)p = v; }
DI void st8(lptr p, v2u v) { *(LAS v2u*)p = v; }
DI float ex2(float x) { return __builtin_amdgcn_exp2f(x); }
DI float rcp(float x) { return __builtin_amdgcn_rcpf(x); }
DI float max3(float a, float b, float c) { return __builtin_fmaxf(__builtin_fmaxf(a, b), c); }
DI float min3(float a, float b, float c) { return __builtin_fminf(__builtin_fminf(a, b), c); }
constexpr int SC1 = 16;
typedef __amdgpu_buffer_rsrc_t rsrc_t;
DI v4u gld16(rsrc_t r, unsigned v, unsigned s) { return __builtin_amdgcn_raw_buffer_load_b128(r, v, s, SC1); }
DI v2u gld8(rsrc_t r, unsigned v, unsigned s) { return __builtin_amdgcn_raw_buffer_load_b64(r, v, s, SC1); }
DI void gst16(rsrc_t r, unsigned v, unsigned s, v4u d) { __builtin_amdgcn_raw_buffer_store_b128(d, r, v, s, SC1); }
DI void gst8(rsrc_t r, unsigned v, unsigned s, v2u d) { __builtin_amdgcn_raw_buffer_store_b64(d, r, v, s, SC1); }
DI int lane_id() {
  int x;
  asm volatile("v_mbcnt_lo_u32_b32 %0, -1, 0\n\tv_mbcnt_hi_u32_b32 %0, -1, %0" : "=v"(x));
  return x;
}
DI v2u load_tag(const char* p) {
  u64 e;
  asm volatile("s_load_dwordx2 %0, %1, 0x0\n\ts_waitcnt lgkmcnt(0)" : "=s"(e) : "s"(p) : "memory");
  e += TAG_STEP;
  return (v2u){(unsigned)e, (unsigned)(e >> 32)};
}
#define LANE_VARS const int l = lane_id(), lg = l >> 4, li = l & 15, lq = (l >> 2) & 3, lp = l & 3; (void)lg; (void)li; (void)lq; (void)lp
DI void poll_flags(rsrc_t ws, unsigned soff, unsigned vo, v2u tag) {
#pragma unroll 1
  for (unsigned it = 0; it < SPIN_CAP; ++it) {
    asm volatile("" ::: "memory");
    v2u f = gld8(ws, vo, soff);
    bool ok = (vo == OOB) || (f[0] == tag[0] && f[1] == tag[1]);
    if (!__builtin_amdgcn_ballot_w64(!ok)) break;
    __builtin_amdgcn_s_sleep(1);
  }
  asm volatile("" ::: "memory");
}
DI void publish_flag(rsrc_t ws, unsigned soff, v2u tag, int l) {
  wait_vm<0>();
  gst8(ws, l == 0 ? 0u : OOB, soff, tag);
}

#define STAMP(i)
#define STAMP2(i)

struct Params {
  const u16 *xA, *wA, *oA, *xB, *wB, *pB, *oB;
  u16* out;
  char* ws;
};

constexpr int SLOT = 20480;
constexpr int XCH = 102400;
constexpr int A_X0 = 81920, A_XS = 36864, A_KB = 117760, A_VB = 140800;
constexpr int B_ST = 53248, B_PHIIMG = 106496, B_QH = 147456, B_R = B_PHIIMG, B_VIMG = 0, B_WPHI = 159744, B_CNT = 162944;
DI constexpr int b_stage(int c) { return ((c + 1) % 3) * B_ST; }
DI constexpr int b_slot(int n) { return (n < 5 ? n : (n < 8 ? n - 3 : 9 - n)) * SLOT; }

template <class T, int N> DI void pin(T (&a)[N]) {
#pragma unroll
  for (int i = 0; i < N; ++i) asm volatile("" : "+v"(a[i]));
}
struct NoHook { template <class T> DI void operator()(T) const {} };

template <class H> DI void feature_map(const v2u (&X)[5], lptr phi_rd, v2u (&P)[10], H&& hook) {
  f4 p[5];
  {
    v2u F[5][5];
#pragma unroll
    for (int t = 0; t < 5; ++t)
#pragma unroll
      for (int u = 0; u < 5; ++u) F[t][u] = trd(phi_rd + t * 160 + u * 8);
    hook(IC<0>{});
#pragma unroll
    for (int u = 0; u < 5; ++u) p[u] = mma4(F[0][u], X[0], (f4){0.f, 0.f, 0.f, 0.f});
#pragma unroll
    for (int t = 1; t < 5; ++t)
#pragma unroll
      for (int u = 0; u < 5; ++u) p[u] = mma4(F[t][u], X[t], p[u]);
  }
  hook(IC<1>{});
  asm volatile("s_nop 7" : "+v"(p[0]), "+v"(p[1]), "+v"(p[2]), "+v"(p[3]), "+v"(p[4]));
  float mx, mn;
  asm("v_max3_f32 %0, %1, %2, %3" : "=v"(mx) : "v"(p[0][0]), "v"(p[0][1]), "v"(p[0][2]));
  asm("v_min3_f32 %0, %1, %2, %3" : "=v"(mn) : "v"(p[0][0]), "v"(p[0][1]), "v"(p[0][2]));
  asm("v_max_f32 %0, %0, %1" : "+v"(mx) : "v"(p[0][3]));
  asm("v_min_f32 %0, %0, %1" : "+v"(mn) : "v"(p[0][3]));
#pragma unroll
  for (int u = 1; u < 5; ++u)
#pragma unroll
    for (int e = 0; e < 4; e += 2) {
      asm("v_max3_f32 %0, %0, %1, %2" : "+v"(mx) : "v"(p[u][e]), "v"(p[u][e + 1]));
      asm("v_min3_f32 %0, %0, %1, %2" : "+v"(mn) : "v"(p[u][e]), "v"(p[u][e + 1]));
    }
  constexpr float L2E = 1.4426950408889634f;
  const float cp = -mx * L2E, cn = mn * L2E;
  f4 ep[5], en[5];
  f2 sp = {0.f, 0.f}, sn = {0.f, 0.f};
  sfor<5>([&](auto U) {
    constexpr int u = U;
    hook(IC<2 + u>{});
    const f4 ap = p[u] * L2E + cp, an = p[u] * (-L2E) + cn;
#pragma unroll
    for (int e = 0; e < 4; ++e) { ep[u][e] = ex2(ap[e]); en[u][e] = ex2(an[e]); }
    sp += (f2){ep[u][0], ep[u][1]} + (f2){ep[u][2], ep[u][3]};
    sn += (f2){en[u][0], en[u][1]} + (f2){en[u][2], en[u][3]};
  });
  hook(IC<7>{});
  const float ip = rcp(sp[0] + sp[1]), in = rcp(sn[0] + sn[1]);
#pragma unroll
  for (int u = 0; u < 5; ++u) P[u] = pk4(ep[u] * ip);
  hook(IC<8>{});
#pragma unroll
  for (int u = 0; u < 5; ++u) P[5 + u] = pk4(en[u] * in);
  hook(IC<9>{});
}

template <bool SWAP, bool PF, int TS>
DI void kstep(const v4u (&cur)[5], v4u x0, v4u x1, f4 (&acc)[2][5], v4u (&nxt)[5], lptr p0, lptr p1) {
#pragma unroll
  for (int t = 0; t < 5; ++t) {
    v2u lo, hi;
    acc[0][t] = SWAP ? mma(cur[t], x0, acc[0][t]) : mma(x0, cur[t], acc[0][t]);
    if (PF) lo = trd(p0 + t * TS);
    SB();
    acc[1][t] = SWAP ? mma(cur[t], x1, acc[1][t]) : mma(x1, cur[t], acc[1][t]);
    if (PF) { hi = trd(p1 + t * TS); nxt[t] = cat(lo, hi); }
    SB();
  }
}
template <int TS> DI void read_w(v4u (&f)[5], lptr p0, lptr p1) {
#pragma unroll
  for (int t = 0; t < 5; ++t) f[t] = cat(trd(p0 + t * TS), trd(p1 + t * TS));
}
DI void zero_acc(f4 (&acc)[2][5]) {
#pragma unroll
  for (int j = 0; j < 2; ++j)
#pragma unroll
    for (int t = 0; t < 5; ++t) acc[j][t] = (f4){0.f, 0.f, 0.f, 0.f};
}
template <int N> DI void arrive(unsigned base, unsigned one) {
  asm volatile("s_mov_b64 exec, 1\n\tds_add_u32 %0, %1 offset:%2\n\ts_mov_b64 exec, -1" :: "v"(base), "v"(one), "n"(4 * N) : "memory");
}

template <int N, int NEED> DI void lds_wait(unsigned base, unsigned cv) {
  unsigned s, it;
  asm volatile(
      "s_nop 0\n\tv_readfirstlane_b32 %1, %0\n\ts_cmp_ge_u32 %1, %4\n\ts_cbranch_scc1 .Lw1_%=\n\t"
      "s_mov_b32 %2, 0x100000\n"
      ".Lw0_%=:\n\tds_read_b32 %0, %3 offset:%5\n\ts_waitcnt lgkmcnt(0)\n\tv_readfirstlane_b32 %1, %0\n\t"
      "s_cmp_ge_u32 %1, %4\n\ts_cbranch_scc1 .Lw1_%=\n\ts_sub_u32 %2, %2, 1\n\ts_cbranch_scc0 .Lw0_%=\n"
      ".Lw1_%=:"
      : "+v"(cv), "=&s"(s), "=&s"(it) : "v"(base), "n"(NEED), "n"(4 * N) : "scc", "memory");
}

__global__ __launch_bounds__(512, 2) void fused_kernel(Params prm) {
  __shared__ __attribute__((aligned(1024))) char smem_[163840];
  const lptr sm = (lptr)smem_;
  const unsigned sbase = (unsigned)(size_t)smem_;
  STAMP(0);
  const int tid = __builtin_amdgcn_workitem_id_x();
  const int w = rfl(tid >> 6), m = w & 3, set = w >> 2;
  const int bid = __builtin_amdgcn_workgroup_id_x(), xcd = bid & 7, idx = bid >> 3;
  const int br = xcd >> 2, rl = idx >> 2, gl = idx & 3;
  const int r = ((xcd & 3) >> 1) * 8 + rl, g = (xcd & 1) * 4 + gl;
  const int q0 = (((rl + gl) % NROT) * 5) / NROT;
  const int mt = 2 * m + (set ^ 1);
  const int dom = (br * 16 + r) * 4 + m;
  const rsrc_t ws = __builtin_amdgcn_make_buffer_rsrc(prm.ws, 0, WS_END, 0x00020000);
  const v4i rx = mk_srd(br ? prm.xB : prm.xA, 2048 * 640 * 2);
  const v4i rw = mk_srd(br ? prm.wB : prm.wA, 640 * 1920 * 2);
  const v4i ro = mk_srd(br ? prm.oB : prm.oA, 640 * 640 * 2);
  unsigned xoff[4], woff[5];
  unsigned kc[5];
#pragma unroll
  for (int c = 0; c < 5; ++c) { int q = q0 + c; kc[c] = q >= 5 ? q - 5 : q; }
  const unsigned xstep = br ? 16 * 1280 : 32 * 1280;
  const unsigned sbx = sbase + m * 8192, sbw = sbase + m * 5120;
  auto issue_x = [&](auto C, auto DST) {
    constexpr int c = C, dst = DST;
    sfor<8>([&](auto I) { constexpr int i = I; dma<dst + i * 1024>(rx, xoff[i & 3], kc[c] * 256 + (i >> 2) * xstep, sbx); });
  };
  auto issue_w1 = [&](auto C, auto PART, auto DST, auto I) {
    constexpr int c = C, part = PART, dst = DST, i = I;
    dma<dst + i * 1024>(rw, woff[i], kc[c] * (128 * 3840) + part * 1280, sbw);
  };
  auto issue_w = [&](auto C, auto PART, auto DST) { sfor<5>([&](auto I) { issue_w1(C, PART, DST, I); }); };
  auto issue_o1 = [&](auto BB, auto I) {
    constexpr int b = BB, i = I;
    dma<b * SLOT + i * 1024>(ro, woff[i], b * 128 * 1280, sbw);
  };
  auto issue_o = [&](auto BB) { sfor<5>([&](auto I) { issue_o1(BB, I); }); };
  auto set_xoff = [&](int lg, int li) {
#pragma unroll
    for (int i = 0; i < 4; ++i) {
      const int rho = 4 * i + lg;
      const int grow = br ? (r * 128 + 32 * m + rho) : (r * 128 + 64 * (m >> 1) + 2 * rho + (m & 1));
      xoff[i] = grow * 1280 + ((li ^ rho) << 4);
    }
  };
  auto set_woff = [&](int l) {
#pragma unroll
    for (int i = 0; i < 5; ++i) {
      const int p = (5 * m + i) * 64 + l, rho = p / 10, c = p - rho * 10;
      woff[i] = (rho ^ (((rho >> 3) & 1) << 2)) * 3840 + c * 16 + g * 160;
    }
  };
  auto set_ooff = [&](int l) {
#pragma unroll
    for (int i = 0; i < 5; ++i) {
      const int p = (5 * m + i) * 64 + l, rho = p / 10, c = p - rho * 10;
      woff[i] = rho * 1280 + c * 16 + g * 160;
    }
  };
  v2u tag3, tag2 = {0u, 0u};
  v2u Oq[5];

  if (br == 0) {
    v2u Q[5];
    if (set == 0) {
      const int l = tid & 63, lg = l >> 4, li = l & 15, lq = (l >> 2) & 3, lp = l & 3;
      set_xoff(lg, li);
      const int pos = 4 * m + lg;
      const int hrow = r * 128 + (pos < 8 ? 2 * pos - 15 : 2 * pos - 32);
      const unsigned hoff = hrow < 0 ? OOB : hrow * 1280 + ((li ^ pos) << 4);
      auto issue_xa = [&](auto C) {
        constexpr int c = C, dst = A_X0 + (c & 1) * A_XS;
        issue_x(C, IC<dst>{});
        dma<dst + 32768>(rx, hoff, kc[c] * 256, sbase + m * 1024);
      };
      auto issue_wa = [&](auto N) { constexpr int n = N; issue_w(IC<n / 3>{}, IC<n % 3>{}, IC<(n & 3) * SLOT>{}); };
      issue_xa(IC<0>{});
      set_woff(l);
      issue_wa(IC<0>{});
      issue_wa(IC<1>{});
      tag3 = load_tag(prm.ws + WS_E3 + (dom << 7));
      const int wr0 = (8 * lg + 4 * (lg & 1) + lq) * 160 + lp * 8, wr1 = (8 * lg + 4 * ((lg & 1) ^ 1) + lq) * 160 + lp * 8;
      f4 hacc[2][2];
      hacc[0][0] = hacc[0][1] = hacc[1][0] = hacc[1][1] = (f4){0.f, 0.f, 0.f, 0.f};
      v4u xh[4];
      sfor<15>([&](auto J) {
        constexpr int j = J, c = j / 3, p = j % 3;
        wait_vm<(j == 14 ? 5 : (p == 2 ? 9 : 5))>();
        STAMP(16 + j);
        bar();
        if constexpr (j == 0) STAMP(1);
        if constexpr (p == 0) issue_wa(IC<j + 2>{});
        if constexpr (p == 1 && c < 4) issue_xa(IC<c + 1>{});
        if constexpr (p == 2 && c < 4) { issue_wa(IC<j + 1>{}); issue_wa(IC<j + 2>{}); }
        if constexpr (j == 13) { set_ooff(l); issue_o(IC<3>{}); }
        if constexpr (j == 14) {
#pragma unroll
          for (int t = 0; t < 5; ++t) Q[t] = ld8(opq(sm + A_VB + mt * 2560 + l * 8) + t * 512);
          issue_o(IC<0>{});
          bar();
        }
        if constexpr (p == 0) {
#pragma unroll
          for (int ks = 0; ks < 4; ++ks)
            xh[ks] = ld16(opq(sm + A_X0 + (c & 1) * A_XS + 32768 + li * 256 + (((4 * ks + lg) ^ li) << 4)));
        } else {
          const lptr b0 = opq(sm + (j & 3) * SLOT + wr0), b1 = opq(sm + (j & 3) * SLOT + wr1);
          const lptr c0 = opq(b0 + m * 32), c1 = opq(b1 + m * 32);
#pragma unroll
          for (int ks = 0; ks < 4; ++ks) {
            const v4u wa = cat(trd(c0 + ks * 5120), trd(c1 + ks * 5120));
            const v4u wb = cat(trd(b0 + ks * 5120 + 128), trd(b1 + ks * 5120 + 128));
            hacc[p - 1][0] = p == 1 ? mma(wa, xh[ks], hacc[p - 1][0]) : mma(xh[ks], wa, hacc[p - 1][0]);
            hacc[p - 1][1] = p == 1 ? mma(wb, xh[ks], hacc[p - 1][1]) : mma(xh[ks], wb, hacc[p - 1][1]);
          }
          if constexpr (j >= 13) {
            const lptr hb = opq(sm + (p == 1 ? A_KB : A_VB) + 40 * 512 + l * 8);
            st8(hb + m * 512, pk4(hacc[p - 1][0]));
            st8(hb + 4 * 512, pk4(hacc[p - 1][1]));
          }
        }
      });
    } else {
      const int l = tid & 63, lg = l >> 4, li = l & 15, lq = (l >> 2) & 3, lp = l & 3;
      tag3 = load_tag(prm.ws + WS_E3 + (dom << 7));
      const int wr0 = (8 * lg + 4 * (lg & 1) + lq) * 160 + lp * 8, wr1 = (8 * lg + 4 * ((lg & 1) ^ 1) + lq) * 160 + lp * 8;
      const int xrd = (32 * m + li) * 256;
      f4 acc[3][2][5];
      zero_acc(acc[0]); zero_acc(acc[1]); zero_acc(acc[2]);
      v4u xf[4][2], wf[2][5];
      auto read_x1 = [&](int c, int ks) {
        const lptr b = opq(sm + A_X0 + (c & 1) * A_XS + xrd + (((4 * ks + lg) ^ li) << 4));
        xf[ks][0] = ld16(b);
        xf[ks][1] = ld16(b + 4096);
      };
      barx();
      STAMP(1);
      read_x1(0, 0);
      read_w<32>(wf[0], opq(sm + wr0), opq(sm + wr1));
      sfor<15>([&](auto J) {
        constexpr int j = J, c = j / 3, p = j % 3;
        const lptr b0 = opq(sm + (j & 3) * SLOT + wr0), b1 = opq(sm + (j & 3) * SLOT + wr1);
        SB();
        sfor<3>([&](auto KS) {
          constexpr int ks = KS;
          if constexpr (p == 0) { read_x1(c, ks + 1); SB(); }
          kstep<(p < 2), true, 32>(wf[ks & 1], xf[ks][0], xf[ks][1], acc[p], wf[(ks + 1) & 1], b0 + (ks + 1) * 5120, b1 + (ks + 1) * 5120);
        });
        if constexpr (j < 14) {
          STAMP2(17 + j);
          if constexpr (j == 13) bar(); else barx();
          const lptr n0 = opq(sm + ((j + 1) & 3) * SLOT + wr0), n1 = opq(sm + ((j + 1) & 3) * SLOT + wr1);
          if constexpr (p == 2) read_x1(c + 1, 0);
          if constexpr (j == 13) {
#pragma unroll
            for (int t = 0; t < 5; ++t) Q[t] = ld8(opq(sm + A_VB + mt * 2560 + l * 8) + t * 512);
          }
          SB();
          kstep<(p < 2), true, 32>(wf[1], xf[3][0], xf[3][1], acc[p], wf[0], n0, n1);
        } else {
          kstep<false, false, 32>(wf[1], xf[3][0], xf[3][1], acc[p], wf[0], b0, b1);
        }
        if constexpr (j >= 12) {
          const lptr ob = opq(sm + (p == 1 ? A_KB : A_VB) + m * 5120 + l * 8);
#pragma unroll
          for (int jj = 0; jj < 2; ++jj)
#pragma unroll
            for (int t = 0; t < 5; ++t) st8(ob + (jj * 5 + t) * 512, pk4(acc[p][jj][t]));
          if constexpr (j == 13) bar();
          SB();
        }
      });
    }
    STAMP(2);
    LANE_VARS;
    const int pv = (mt & 1) ? mt - 1 : (m >= 2 ? mt - 3 : 8);
    v4u P[4];
    {
      v2u K[2][5];
      const lptr kc0 = opq(sm + A_KB + mt * 2560 + l * 8), kp0 = opq(sm + A_KB + pv * 2560 + l * 8);
#pragma unroll
      for (int t = 0; t < 5; ++t) { K[0][t] = ld8(kc0 + t * 512); K[1][t] = ld8(kp0 + t * 512); }
      f4 mc, mp;
      v2u selc;
      const bool halo = pv == 8, oddh = halo && (m & 1);
      const int plo = (halo && r == 0) ? 99 : (oddh ? li + 1 : li + 9), phi = oddh ? 7 : 15;
#pragma unroll
      for (int rr = 0; rr < 4; ++rr) {
        const int pos = 4 * lg + rr;
        mc[rr] = (pos <= li && pos >= li - 7) ? 0.f : -1e30f;
        mp[rr] = (pos >= plo && pos <= phi) ? 0.f : -1e30f;
      }
      selc[0] = (4 * lg <= li ? 0xffffu : 0u) | (4 * lg + 1 <= li ? 0xffff0000u : 0u);
      selc[1] = (4 * lg + 2 <= li ? 0xffffu : 0u) | (4 * lg + 3 <= li ? 0xffff0000u : 0u);
      constexpr float CS = 0.22360679774997896f * 1.4426950408889634f;
#pragma unroll
      for (int h = 0; h < 4; ++h) {
        const v2u z = {0u, 0u};
        const v4u qh = cat(lg >= h ? Q[h] : z, lg <= h ? Q[h + 1] : z);
        const f4 sc = mma(cat(K[0][h], K[0][h + 1]), qh, mc);
        const f4 sp = mma(cat(K[1][h], K[1][h + 1]), qh, mp);
        f4 s;
#pragma unroll
        for (int rr = 0; rr < 4; ++rr) s[rr] = __builtin_fmaxf(sc[rr], sp[rr]);
        float mx = max3(s[0], s[1], __builtin_fmaxf(s[2], s[3]));
        {
          auto a = __builtin_amdgcn_permlane32_swap(f2u(mx), f2u(mx), false, false);
          mx = __builtin_fmaxf(u2f(a[0]), u2f(a[1]));
          auto b = __builtin_amdgcn_permlane16_swap(f2u(mx), f2u(mx), false, false);
          mx = __builtin_fmaxf(u2f(b[0]), u2f(b[1]));
        }
        f4 e;
#pragma unroll
        for (int rr = 0; rr < 4; ++rr) e[rr] = ex2((s[rr] - mx) * CS);
        float sum = (e[0] + e[1]) + (e[2] + e[3]);
        {
          auto a = __builtin_amdgcn_permlane32_swap(f2u(sum), f2u(sum), false, false);
          sum = u2f(a[0]) + u2f(a[1]);
          auto b = __builtin_amdgcn_permlane16_swap(f2u(sum), f2u(sum), false, false);
          sum = u2f(b[0]) + u2f(b[1]);
        }
        const v2u pp = pk4(e * rcp(sum));
        P[h] = (v4u){pp[0] & selc[0], pp[1] & selc[1], pp[0] & ~selc[0], pp[1] & ~selc[1]};
      }
    }
    pin(P);
    STAMP(3);
    bar();
    const lptr vc0 = opq(sm + A_VB + mt * 2560 + l * 8), vp0 = opq(sm + A_VB + pv * 2560 + l * 8);
#pragma unroll
    for (int n = 0; n < 5; ++n) {
      const v4u vf = cat(ld8(vc0 + n * 512), ld8(vp0 + n * 512));
      const f4 zz = {0.f, 0.f, 0.f, 0.f};
      f4 o;
      if (n == 0) o = mma(vf, P[0], zz);
      else if (n == 4) o = mma(vf, P[3], zz);
      else {
        const f4 hi = mma(vf, P[n], zz), lo = mma(vf, P[n - 1], zz);
        o = lg >= n ? hi : lo;
      }
      Oq[n] = pk4(o);
    }
  } else {
    volatile LAS unsigned* cnt = (volatile LAS unsigned*)(sm + B_CNT);
    v2u Xq[5];
    if (set == 1) {
      v4u xr[5][4][2];
      int wr0, wr1;
      {
        const int l = tid & 63, lg = l >> 4, li = l & 15, lq = (l >> 2) & 3, lp = l & 3;
        set_xoff(lg, li);
        issue_x(IC<0>{}, IC<b_stage(0)>{});
        set_woff(l);
        issue_w(IC<0>{}, IC<1>{}, IC<b_stage(0) + 32768>{});
        issue_x(IC<1>{}, IC<b_stage(1)>{});
        tag3 = load_tag(prm.ws + WS_E3 + (dom << 7));
        wr0 = (8 * lg + 4 * (lg & 1) + lq) * 160 + lp * 40;
        wr1 = (8 * lg + 4 * ((lg & 1) ^ 1) + lq) * 160 + lp * 40;
        const int xrd = (32 * m + li) * 256;
        sfor<5>([&](auto C) {
          constexpr int c = C;
          wait_vm<(c < 4 ? 8 : 5)>();
          STAMP(16 + c);
          bar();
          if constexpr (c == 0) STAMP(1);
#pragma unroll
          for (int ks = 0; ks < 4; ++ks) {
            const lptr b = opq(sm + b_stage(c) + xrd + (((4 * ks + lg) ^ li) << 4));
            xr[c][ks][0] = ld16(b);
            xr[c][ks][1] = ld16(b + 4096);
          }
          if constexpr (c < 4) issue_w(IC<c + 1>{}, IC<1>{}, IC<b_stage(c + 1) + 32768>{});
          if constexpr (c < 3) issue_x(IC<c + 2>{}, IC<b_stage(c + 2)>{});
          if constexpr (c >= 3) issue_w(IC<c - 3>{}, IC<2>{}, IC<b_slot(c - 3)>{});
        });
      }
      wait_vm<5>();
      bar();
      STAMP(2);
      f4 acc[2][5];
      v4u wf[2][5];
      auto spin = [&](auto N, unsigned cv) {
        constexpr int n = N;
        lds_wait<n, ((n >= 2 && n <= 5) ? 8 : 4)>(sbase + B_CNT, cv);
      };
      sfor<10>([&](auto N) {
        constexpr int n = N;
        const lptr b0 = opq(sm + b_slot(n) + wr0), b1 = opq(sm + b_slot(n) + wr1);
        if constexpr (n == 5) {
          issue_w(IC<0>{}, IC<0>{}, IC<b_slot(5)>{});
          issue_w(IC<1>{}, IC<0>{}, IC<b_slot(6)>{});
          issue_w(IC<2>{}, IC<0>{}, IC<b_slot(7)>{});
          wait_vm<10>();
          arrive<5>(sbase + B_CNT, 1u);
          spin(IC<5>{}, 0u);
        }
        if constexpr (n % 5 == 0) {
          zero_acc(acc);
          read_w<8>(wf[0], b0, b1);
        }
        if constexpr (n == 7) set_ooff(lane_id());
        auto paced = [&](auto I) {
          if constexpr (n == 5 || n == 6) issue_w1(IC<n - 2>{}, IC<0>{}, IC<b_slot(n + 3)>{}, I);
          if constexpr (n >= 7) issue_o1(IC<n - 5>{}, I);
          SB();
        };
        constexpr bool more = n % 5 != 4;
        unsigned cv = 0;
        SB();
        paced(IC<0>{});
        kstep<true, true, 8>(wf[0], xr[n % 5][0][0], xr[n % 5][0][1], acc, wf[1], b0 + 5120, b1 + 5120);
        if constexpr (more && n > 0) {
          if constexpr (n >= 5) wait_vm<6>();
          arrive<n + 1>(sbase + B_CNT, 1u);
        }
        paced(IC<1>{});
        kstep<true, true, 8>(wf[1], xr[n % 5][1][0], xr[n % 5][1][1], acc, wf[0], b0 + 10240, b1 + 10240);
        if constexpr (more && n > 0) cv = cnt[n + 1];
        paced(IC<2>{});
        kstep<true, true, 8>(wf[0], xr[n % 5][2][0], xr[n % 5][2][1], acc, wf[1], b0 + 15360, b1 + 15360);
        if constexpr (more) {
          if constexpr (n == 0) {
            wait_vm<0>();
            arrive<1>(sbase + B_CNT, 1u);
          }
          spin(IC<n + 1>{}, cv);
          STAMP2(22 + n);
          const lptr n0 = opq(sm + b_slot(n + 1) + wr0), n1 = opq(sm + b_slot(n + 1) + wr1);
          paced(IC<3>{});
          kstep<true, true, 8>(wf[1], xr[n % 5][3][0], xr[n % 5][3][1], acc, wf[0], n0, n1);
        } else {
          paced(IC<3>{});
          kstep<true, false, 8>(wf[1], xr[n % 5][3][0], xr[n % 5][3][1], acc, wf[0], b0, b1);
        }
        paced(IC<4>{});
        if constexpr (n == 4) {
          LANE_VARS;
          const lptr vp = opq(sm + B_VIMG + lg * 6144 + (32 * m + li) * 48);
#pragma unroll
          for (int j = 0; j < 2; ++j) {
            st16(vp + j * 768, cat(pk4(acc[j][0]), pk4(acc[j][1])));
            st16(vp + j * 768 + 16, cat(pk4(acc[j][2]), pk4(acc[j][3])));
            st16(vp + j * 768 + 32, cat(pk4(acc[j][4]), (v2u){0x3f80u, 0u}));
          }
          STAMP(3);
          bar();
        }
      });
      const lptr qh = opq(sm + B_QH + m * 2560 + lane_id() * 8);
#pragma unroll
      for (int t = 0; t < 5; ++t) {
        Xq[t] = pk4(acc[0][t]);
        st8(qh + t * 512, pk4(acc[1][t]));
      }
      STAMP(4);
      bar();
    } else {
      const int l = tid & 63, lg = l >> 4, li = l & 15, lq = (l >> 2) & 3, lp = l & 3;
      {
        const v4i rp = mk_srd(prm.pB, 32 * 400 * 2);
        if (l < 50) dma<B_WPHI>(rp, l * 16, (g * 4 + m) * 800, sbase + m * 800);
      }
      tag3 = load_tag(prm.ws + WS_E3 + (dom << 7));
      tag2 = load_tag(prm.ws + WS_E2 + (g << 7));
      if (w == 0 && l < 16) cnt[l] = 0;
      set_woff(l);
      const int wr0 = (8 * lg + 4 * (lg & 1) + lq) * 160 + lp * 40, wr1 = (8 * lg + 4 * ((lg & 1) ^ 1) + lq) * 160 + lp * 40;
      const int xrd = (32 * m + li) * 256;
      f4 acc[2][5];
      zero_acc(acc);
      sfor<5>([&](auto C) {
        constexpr int c = C;
        if constexpr (c == 4) wait_vm<0>();
        STAMP(16 + c);
        bar();
        if constexpr (c == 0) STAMP(1);
        v4u xf[4][2], wf[2][5];
        const lptr b0 = opq(sm + b_stage(c) + 32768 + wr0), b1 = opq(sm + b_stage(c) + 32768 + wr1);
        auto rx1 = [&](int ks) {
          const lptr b = opq(sm + b_stage(c) + xrd + (((4 * ks + lg) ^ li) << 4));
          xf[ks][0] = ld16(b);
          xf[ks][1] = ld16(b + 4096);
        };
        auto paced = [&](auto I) {
          if constexpr (c == 4) issue_w1(IC<2>{}, IC<2>{}, IC<b_slot(2)>{}, I);
          SB();
        };
        rx1(0);
        read_w<8>(wf[0], b0, b1);
        rx1(1);
        paced(IC<0>{});
        kstep<true, true, 8>(wf[0], xf[0][0], xf[0][1], acc, wf[1], b0 + 5120, b1 + 5120);
        rx1(2);
        paced(IC<1>{});
        kstep<true, true, 8>(wf[1], xf[1][0], xf[1][1], acc, wf[0], b0 + 10240, b1 + 10240);
        rx1(3);
        paced(IC<2>{});
        kstep<true, true, 8>(wf[0], xf[2][0], xf[2][1], acc, wf[1], b0 + 15360, b1 + 15360);
        paced(IC<3>{});
        kstep<true, false, 8>(wf[1], xf[3][0], xf[3][1], acc, wf[0], b0, b1);
        paced(IC<4>{});
      });
      bar();
      STAMP(2);
      sfor<2>([&](auto JJ) {
        constexpr int j = JJ;
        v2u X[5], P[10];
#pragma unroll
        for (int t = 0; t < 5; ++t) X[t] = pk4(acc[j][t]);
        feature_map(X, opq(sm + B_WPHI + lg * 800 + lq * 40), P, [&](auto I) {
          constexpr int i = I;
          SB();
          if constexpr (j == 0) {
            if constexpr (i == 5) { wait_vm<5>(); arrive<2>(sbase + B_CNT, 1u); }
            issue_w1(IC<3 + i / 5>{}, IC<2>{}, IC<b_slot(3 + i / 5)>{}, IC<i % 5>{});
          } else {
            if constexpr (i == 0) { wait_vm<5>(); arrive<3>(sbase + B_CNT, 1u); }
            if constexpr (i == 4) { wait_vm<0>(); arrive<4>(sbase + B_CNT, 1u); }
          }
          SB();
        });
        const lptr rp = opq(sm + B_PHIIMG + lg * 10240 + (32 * m + 16 * j + li) * 80);
#pragma unroll
        for (int c = 0; c < 5; ++c) st16(rp + c * 16, cat(P[2 * c], P[2 * c + 1]));
      });
      STAMP(3);
      bar();
      STAMP(4);
      {
        f4 d0[3], d1[3];
#pragma unroll
        for (int b = 0; b < 3; ++b) d0[b] = d1[b] = (f4){0.f, 0.f, 0.f, 0.f};
        const lptr fp = opq(sm + B_PHIIMG + m * 10240 + (4 * lg + lq) * 80 + lp * 8);
        const lptr dp = opq(sm + B_VIMG + m * 6144 + (4 * lg + lq) * 48 + lp * 8);
#pragma unroll
        for (int ks = 0; ks < 4; ++ks) {
          v4u F[3], D[2];
#pragma unroll
          for (int a = 0; a < 2; ++a) D[a] = cat(trd(dp + ks * 1536 + a * 32), trd(dp + ks * 1536 + 768 + a * 32));
#pragma unroll
          for (int b = 0; b < 3; ++b) F[b] = cat(trd(fp + ks * 2560 + b * 32), trd(fp + ks * 2560 + 1280 + b * 32));
#pragma unroll
          for (int b = 0; b < 3; ++b) {
            d0[b] = mma(D[0], F[b], d0[b]);
            d1[b] = mma(D[1], F[b], d1[b]);
          }
        }
        arrive<5>(sbase + B_CNT, 1u);
        const unsigned so = WS_KP + (g * 4 + m) * 9 * 8192 + r * 512;
        const unsigned v0 = (l >> 5) * 8192 + (l & 31) * 16, v1 = l < 32 ? 2 * 8192 + l * 16 : OOB;
#pragma unroll
        for (int b = 0; b < 3; ++b) {
          gst16(ws, v0, so + b * 3 * 8192, __builtin_bit_cast(v4u, d0[b]));
          gst16(ws, v1, so + b * 3 * 8192, __builtin_bit_cast(v4u, d1[b]));
        }
        STAMP(5);
        publish_flag(ws, WS_F1 + (g * 4 + m) * 128 + r * 8, tag2, l);
        STAMP(6);
      }
      const int rid = m * 16 + r;
      if (rid < 36) {
        const int h = rid / 9;
        poll_flags(ws, WS_F1 + (g * 4 + h) * 128, l < 16 ? l * 8u : OOB, tag2);
        STAMP(7);
        f4 s = {0.f, 0.f, 0.f, 0.f};
        v4u pv[8];
#pragma unroll
        for (int i = 0; i < 8; ++i) pv[i] = gld16(ws, l * 16, WS_KP + (g * 36 + rid) * 8192 + i * 1024);
#pragma unroll
        for (int i = 0; i < 8; ++i) s += __builtin_bit_cast(f4, pv[i]);
#pragma unroll
        for (int e = 0; e < 4; ++e) {
          auto a = __builtin_amdgcn_permlane32_swap(f2u(s[e]), f2u(s[e]), false, false);
          s[e] = u2f(a[0]) + u2f(a[1]);
        }
        gst8(ws, l < 32 ? l * 8 : OOB, WS_KR + (g * 36 + rid) * 256, pk4(s));
        publish_flag(ws, WS_F2 + (g * 4 + h) * 128 + (rid - h * 9) * 8, tag2, l);
        STAMP(8);
      }
      bar();
#pragma unroll
      for (int t = 0; t < 5; ++t) Xq[t] = ld8(opq(sm + B_QH + m * 2560 + l * 8) + t * 512);
    }
    LANE_VARS;
    v2u Pq[10];
    feature_map(Xq, opq(sm + B_WPHI + lg * 800 + lq * 40), Pq, NoHook{});
    pin(Pq);
    STAMP(9);
    if (set == 0) {
      poll_flags(ws, WS_F2 + (g * 4 + m) * 128, l < 9 ? l * 8u : OOB, tag2);
      v4u kr[3];
#pragma unroll
      for (int i = 0; i < 3; ++i)
        kr[i] = gld16(ws, (i < 2 || l < 16) ? l * 16 : OOB, WS_KR + (g * 4 + m) * 2304 + i * 1024);
      const lptr rw_ = opq(sm + B_R + m * 2304 + l * 16);
      st16(rw_, kr[0]);
      st16(rw_ + 1024, kr[1]);
      if (l < 16) st16(rw_ + 2048, kr[2]);
    } else {
      issue_o(IC<1>{});
      issue_o(IC<0>{});
    }
    bar();
    STAMP(10);
    if (w == 0 && r == 0) gst8(ws, l == 0 ? 0u : OOB, WS_E2 + (g << 7), tag2);
    f4 nm[6];
#pragma unroll
    for (int tt = 0; tt < 6; ++tt) nm[tt] = (f4){0.f, 0.f, 0.f, 0.f};
    const lptr r_rd = opq(sm + B_R + lg * 2304 + lq * 8);
#pragma unroll
    for (int u = 0; u < 10; ++u)
#pragma unroll
      for (int tt = 0; tt < 6; ++tt) nm[tt] = mma4(trd(r_rd + (u >> 2) * 768 + tt * 128 + (u & 3) * 32), Pq[u], nm[tt]);
    const float id = rcp(nm[5][0] + 1e-6f);
#pragma unroll
    for (int t = 0; t < 5; ++t) Oq[t] = pk4(nm[t] * id);
  }

  pin(Oq);
  STAMP(11);
  LANE_VARS;
  {
    const unsigned so = WS_O + ((dom * 2 + (set ^ 1)) * 8 + g) * 2560;
    gst16(ws, l * 16, so, cat(Oq[0], Oq[1]));
    gst16(ws, l * 16, so + 1024, cat(Oq[2], Oq[3]));
    gst8(ws, l * 8, so + 2048, Oq[4]);
    if (br == 0 && set == 0) {
      issue_o(IC<4>{});
      wait_vm<5>();
      gst8(ws, l == 0 ? 0u : OOB, WS_F3 + (dom << 7) + 64 + g * 8, tag3);
      issue_o(IC<1>{});
      issue_o(IC<2>{});
      wait_vm<0>();
    } else {
      publish_flag(ws, WS_F3 + (dom << 7) + (set ^ 1) * 64 + g * 8, tag3, l);
    }
  }
  STAMP(12);
  bar();
  f4 acc[2][5];
  zero_acc(acc);
  auto gemm2 = [&](auto BRC) {
    constexpr int TS = BRC ? 4 : 16;
    const lptr wo_rd = opq(sm + set * (320 * 160) + ((BRC ? 20 : 4) * lg + lq) * 160 + lp * 40);
    auto r0 = [](int s) { const int k = s % 5; return 160 * (s / 5) + (k < 4 ? 80 * (k >> 1) + TS * 2 * (k & 1) : TS * 4); };
    auto r1 = [&](int s) { return r0(s) + (s % 5 < 4 ? TS : 80); };
    v4u wf[2][5];
    read_w<8>(wf[0], wo_rd + r0(0) * 160, wo_rd + r1(0) * 160);
    poll_flags(ws, WS_F3 + (dom << 7) + set * 32, l < 8 ? (l >> 2) * 64u + (l & 3) * 8u : OOB, tag3);
    STAMP(13);
    v4u of[10][2];
#pragma unroll
    for (int s = 0; s < 10; ++s)
#pragma unroll
      for (int jj = 0; jj < 2; ++jj) {
        const unsigned so = WS_O + ((dom * 2 + jj) * 8 + 4 * set + 2 * (s / 5)) * 2560;
        const int k = s % 5;
        of[s][jj] = k < 4 ? gld16(ws, l * 16, so + (k >> 1) * 2560 + (k & 1) * 1024)
                          : cat(gld8(ws, l * 8, so + 2048), gld8(ws, l * 8, so + 2560 + 2048));
      }
    SB();
    sfor<10>([&](auto S) {
      constexpr int s = S, sn = s < 9 ? s + 1 : s;
      kstep<true, (s < 9), 8>(wf[s & 1], of[s][0], of[s][1], acc, wf[(s + 1) & 1], wo_rd + r0(sn) * 160, wo_rd + r1(sn) * 160);
    });
  };
  if (br) gemm2(IC<1>{});
  else gemm2(IC<0>{});
  pin(acc[0]);
  pin(acc[1]);
  STAMP(14);
  f4 res[5];
  {
    const lptr xw = opq(sm + XCH + w * 5120 + l * 16), xr_ = opq(sm + XCH + (w ^ 4) * 5120 + l * 16);
#pragma unroll
    for (int t = 0; t < 5; ++t) {
      st16(xw + t * 1024, __builtin_bit_cast(v4u, set ? acc[1][t] : acc[0][t]));
      res[t] = set ? acc[0][t] : acc[1][t];
    }
    bar();
    if (g == 0 && set == 0) gst8(ws, l == 0 ? 0u : OOB, WS_E3 + (dom << 7), tag3);
#pragma unroll
    for (int t = 0; t < 5; ++t) res[t] += __builtin_bit_cast(f4, ld16(xr_ + t * 1024));
  }
  {
    const int jj = mt & 1;
    const int srow = br ? (r * 128 + 16 * mt + li) : (r * 128 + 64 * (m >> 1) + 2 * (16 * jj + li) + (m & 1));
    const rsrc_t rout = __builtin_amdgcn_make_buffer_rsrc(prm.out, 0, 2048 * 1280 * 2, 0x00020000);
    const unsigned vo = srow * 2560 + lg * 40, so = br * 1280 + g * 160;
    __builtin_amdgcn_raw_buffer_store_b128(cat(pk4(res[0]), pk4(res[1])), rout, vo, so, 0);
    __builtin_amdgcn_raw_buffer_store_b128(cat(pk4(res[2]), pk4(res[3])), rout, vo, so + 16, 0);
    __builtin_amdgcn_raw_buffer_store_b64(pk4(res[4]), rout, vo, so + 32, 0);
  }
}

#if defined(__HIP_DEVICE_COMPILE__)
#pragma clang attribute pop
#endif

extern "C" void kernel_launch(void* const* d_in, const int* in_sizes, int n_in, void* d_out, int out_size, void* d_ws,
                              size_t ws_size, hipStream_t stream) {
  Params p;
  p.xA = (const u16*)d_in[0]; p.wA = (const u16*)d_in[1]; p.oA = (const u16*)d_in[2];
  p.xB = (const u16*)d_in[3]; p.wB = (const u16*)d_in[4]; p.pB = (const u16*)d_in[5]; p.oB = (const u16*)d_in[6];
  p.out = (u16*)d_out; p.ws = (char*)d_ws;
  fused_kernel<<<256, 512, 0, stream>>>(p);
}
```

```cpp
#define PROFILE 1
#include <hip/hip_runtime.h>
#include <hip/hip_bf16.h>
#include <type_traits>

#define NROT 3

#if defined(__HIP_DEVICE_COMPILE__)
#pragma clang attribute push(__attribute__((target("no-packed-fp32-ops"))), apply_to = function)
#endif
typedef unsigned short u16;
typedef unsigned long long u64;
typedef unsigned v2u __attribute__((ext_vector_type(2)));
typedef unsigned v4u __attribute__((ext_vector_type(4)));
typedef int v4i __attribute__((ext_vector_type(4)));
typedef float f2 __attribute__((ext_vector_type(2)));
typedef float f4 __attribute__((ext_vector_type(4)));
typedef short s4 __attribute__((ext_vector_type(4)));
typedef __bf16 bf2 __attribute__((ext_vector_type(2)));
typedef __bf16 bf8 __attribute__((ext_vector_type(8)));
#define LAS __attribute__((address_space(3)))
#define GAS __attribute__((address_space(1)))
#define DI __device__ __forceinline__
typedef LAS char* lptr;

constexpr unsigned WS_O = 0;
constexpr unsigned WS_KP = 5242880;
constexpr unsigned WS_KR = WS_KP + 8 * 36 * 8192;
constexpr unsigned WS_F3 = WS_KR + 8 * 9216;
constexpr unsigned WS_E3 = WS_F3 + 256 * 128;
constexpr unsigned WS_F1 = WS_E3 + 256 * 128;
constexpr unsigned WS_F2 = WS_F1 + 32 * 128;
constexpr unsigned WS_E2 = WS_F2 + 32 * 128;
constexpr unsigned WS_END = WS_E2 + 8 * 128;
constexpr unsigned WS_DBG = 16u << 20;
constexpr unsigned OOB = 0x7f000000u;
constexpr u64 TAG_STEP = 0x9E3779B97F4A7C15ull;
constexpr unsigned SPIN_CAP = 1u << 16;

template <int N> using IC = std::integral_constant<int, N>;
template <int I, int N, class F> DI void sfor_(F&& f) {
  if constexpr (I < N) { f(IC<I>{}); sfor_<I + 1, N>(f); }
}
template <int N, class F> DI void sfor(F&& f) { sfor_<0, N>(f); }

DI unsigned f2u(float x) { return __builtin_bit_cast(unsigned, x); }
DI float u2f(unsigned x) { return __builtin_bit_cast(float, x); }
DI unsigned rfl(unsigned x) { return __builtin_amdgcn_readfirstlane(x); }
DI v4i mk_srd(const void* p, unsigned bytes) {
  u64 a = (u64)p;
  v4i r;
  r.x = rfl((unsigned)a); r.y = rfl((unsigned)(a >> 32) & 0xffffu); r.z = bytes; r.w = 0x00020000;
  return r;
}
template <int IMM> DI void dma(v4i rs, unsigned voff, unsigned soff, unsigned sbase) {
  asm volatile("s_add_u32 m0, %3, %4\n\ts_nop 0\n\tbuffer_load_dwordx4 %0, %1, %2 offen lds"
               :: "v"(voff), "s"(rs), "s"(soff), "s"(sbase), "n"(IMM) : "memory", "scc");
}
template <int N> DI void wait_vm() { asm volatile("s_waitcnt vmcnt(%0)" :: "n"(N) : "memory"); }
DI void bar() { asm volatile("s_waitcnt lgkmcnt(0)\n\ts_barrier" ::: "memory"); }
DI void barx() { asm volatile("s_barrier" ::: "memory"); }
#define SB() __builtin_amdgcn_sched_barrier(0)
DI unsigned pk2(float a, float b) { return __builtin_bit_cast(unsigned, __builtin_convertvector((f2){a, b}, bf2)); }
DI v2u pk4(f4 v) { return (v2u){pk2(v[0], v[1]), pk2(v[2], v[3])}; }
DI v4u cat(v2u a, v2u b) { return (v4u){a[0], a[1], b[0], b[1]}; }
DI f4 mma(v4u a, v4u b, f4 c) {
  return __builtin_amdgcn_mfma_f32_16x16x32_bf16(__builtin_bit_cast(bf8, a), __builtin_bit_cast(bf8, b), c, 0, 0, 0);
}
DI f4 mma4(v2u a, v2u b, f4 c) {
  return __builtin_amdgcn_mfma_f32_4x4x4bf16_1k(__builtin_bit_cast(s4, a), __builtin_bit_cast(s4, b), c, 0, 0, 0);
}
DI v2u trd(lptr p) { return __builtin_bit_cast(v2u, __builtin_amdgcn_ds_read_tr16_b64_v4i16((LAS s4*)p)); }
DI lptr opq(lptr p) { unsigned a = (unsigned)(size_t)p; asm volatile("" : "+v"(a)); return (lptr)(size_t)a; }
DI v4u ld16(lptr p) { return *(LAS v4u*)p; }
DI v2u ld8(lptr p) { return *(LAS v2u*)p; }
DI void st16(lptr p, v4u v) { *(LAS v4u*)p = v; }
DI void st8(lptr p, v2u v) { *(LAS v2u*)p = v; }
DI float ex2(float x) { return __builtin_amdgcn_exp2f(x); }
DI float rcp(float x) { return __builtin_amdgcn_rcpf(x); }
DI float max3(float a, float b, float c) { return __builtin_fmaxf(__builtin_fmaxf(a, b), c); }
DI float min3(float a, float b, float c) { return __builtin_fminf(__builtin_fminf(a, b), c); }
constexpr int SC1 = 16;
typedef __amdgpu_buffer_rsrc_t rsrc_t;
DI v4u gld16(rsrc_t r, unsigned v, unsigned s) { return __builtin_amdgcn_raw_buffer_load_b128(r, v, s, SC1); }
DI v2u gld8(rsrc_t r, unsigned v, unsigned s) { return __builtin_amdgcn_raw_buffer_load_b64(r, v, s, SC1); }
DI void gst16(rsrc_t r, unsigned v, unsigned s, v4u d) { __builtin_amdgcn_raw_buffer_store_b128(d, r, v, s, SC1); }
DI void gst8(rsrc_t r, unsigned v, unsigned s, v2u d) { __builtin_amdgcn_raw_buffer_store_b64(d, r, v, s, SC1); }
DI int lane_id() {
  int x;
  asm volatile("v_mbcnt_lo_u32_b32 %0, -1, 0\n\tv_mbcnt_hi_u32_b32 %0, -1, %0" : "=v"(x));
  return x;
}
DI v2u load_tag(const char* p) {
  u64 e;
  asm volatile("s_load_dwordx2 %0, %1, 0x0\n\ts_waitcnt lgkmcnt(0)" : "=s"(e) : "s"(p) : "memory");
  e += TAG_STEP;
  return (v2u){(unsigned)e, (unsigned)(e >> 32)};
}
#define LANE_VARS const int l = lane_id(), lg = l >> 4, li = l & 15, lq = (l >> 2) & 3, lp = l & 3; (void)lg; (void)li; (void)lq; (void)lp
DI void poll_flags(rsrc_t ws, unsigned soff, unsigned vo, v2u tag) {
#pragma unroll 1
  for (unsigned it = 0; it < SPIN_CAP; ++it) {
    asm volatile("" ::: "memory");
    v2u f = gld8(ws, vo, soff);
    bool ok = (vo == OOB) || (f[0] == tag[0] && f[1] == tag[1]);
    if (!__builtin_amdgcn_ballot_w64(!ok)) break;
    __builtin_amdgcn_s_sleep(1);
  }
  asm volatile("" ::: "memory");
}
DI void publish_flag(rsrc_t ws, unsigned soff, v2u tag, int l) {
  wait_vm<0>();
  gst8(ws, l == 0 ? 0u : OOB, soff, tag);
}

#define STAMP_CLK __builtin_amdgcn_s_memrealtime
#define NST 32
#define PROF_BR(b) (((b) & 7) >> 2)
#define STAMP(i) do { __builtin_amdgcn_sched_barrier(0); stamp[i] = (unsigned)STAMP_CLK(); __builtin_amdgcn_sched_barrier(0); } while (0)
#define STAMP2(i)

struct Params {
  const u16 *xA, *wA, *oA, *xB, *wB, *pB, *oB;
  u16* out;
  char* ws;
};

constexpr int SLOT = 20480;
constexpr int XCH = 102400;
constexpr int A_X0 = 81920, A_XS = 36864, A_KB = 117760, A_VB = 140800;
constexpr int B_ST = 53248, B_PHIIMG = 106496, B_QH = 147456, B_R = B_PHIIMG, B_VIMG = 0, B_WPHI = 159744, B_CNT = 162944;
DI constexpr int b_stage(int c) { return ((c + 1) % 3) * B_ST; }
DI constexpr int b_slot(int n) { return (n < 5 ? n : (n < 8 ? n - 3 : 9 - n)) * SLOT; }

template <class T, int N> DI void pin(T (&a)[N]) {
#pragma unroll
  for (int i = 0; i < N; ++i) asm volatile("" : "+v"(a[i]));
}
struct NoHook { template <class T> DI void operator()(T) const {} };

template <class H> DI void feature_map(const v2u (&X)[5], lptr phi_rd, v2u (&P)[10], H&& hook) {
  f4 p[5];
  {
    v2u F[5][5];
#pragma unroll
    for (int t = 0; t < 5; ++t)
#pragma unroll
      for (int u = 0; u < 5; ++u) F[t][u] = trd(phi_rd + t * 160 + u * 8);
    hook(IC<0>{});
#pragma unroll
    for (int u = 0; u < 5; ++u) p[u] = mma4(F[0][u], X[0], (f4){0.f, 0.f, 0.f, 0.f});
#pragma unroll
    for (int t = 1; t < 5; ++t)
#pragma unroll
      for (int u = 0; u < 5; ++u) p[u] = mma4(F[t][u], X[t], p[u]);
  }
  hook(IC<1>{});
  asm volatile("s_nop 7" : "+v"(p[0]), "+v"(p[1]), "+v"(p[2]), "+v"(p[3]), "+v"(p[4]));
  float mx, mn;
  asm("v_max3_f32 %0, %1, %2, %3" : "=v"(mx) : "v"(p[0][0]), "v"(p[0][1]), "v"(p[0][2]));
  asm("v_min3_f32 %0, %1, %2, %3" : "=v"(mn) : "v"(p[0][0]), "v"(p[0][1]), "v"(p[0][2]));
  asm("v_max_f32 %0, %0, %1" : "+v"(mx) : "v"(p[0][3]));
  asm("v_min_f32 %0, %0, %1" : "+v"(mn) : "v"(p[0][3]));
#pragma unroll
  for (int u = 1; u < 5; ++u)
#pragma unroll
    for (int e = 0; e < 4; e += 2) {
      asm("v_max3_f32 %0, %0, %1, %2" : "+v"(mx) : "v"(p[u][e]), "v"(p[u][e + 1]));
      asm("v_min3_f32 %0, %0, %1, %2" : "+v"(mn) : "v"(p[u][e]), "v"(p[u][e + 1]));
    }
  constexpr float L2E = 1.4426950408889634f;
  const float cp = -mx * L2E, cn = mn * L2E;
  f4 ep[5], en[5];
  f2 sp = {0.f, 0.f}, sn = {0.f, 0.f};
  sfor<5>([&](auto U) {
    constexpr int u = U;
    hook(IC<2 + u>{});
    const f4 ap = p[u] * L2E + cp, an = p[u] * (-L2E) + cn;
#pragma unroll
    for (int e = 0; e < 4; ++e) { ep[u][e] = ex2(ap[e]); en[u][e] = ex2(an[e]); }
    sp += (f2){ep[u][0], ep[u][1]} + (f2){ep[u][2], ep[u][3]};
    sn += (f2){en[u][0], en[u][1]} + (f2){en[u][2], en[u][3]};
  });
  hook(IC<7>{});
  const float ip = rcp(sp[0] + sp[1]), in = rcp(sn[0] + sn[1]);
#pragma unroll
  for (int u = 0; u < 5; ++u) P[u] = pk4(ep[u] * ip);
  hook(IC<8>{});
#pragma unroll
  for (int u = 0; u < 5; ++u) P[5 + u] = pk4(en[u] * in);
  hook(IC<9>{});
}

template <bool SWAP, bool PF, int TS>
DI void kstep(const v4u (&cur)[5], v4u x0, v4u x1, f4 (&acc)[2][5], v4u (&nxt)[5], lptr p0, lptr p1) {
#pragma unroll
  for (int t = 0; t < 5; ++t) {
    v2u lo, hi;
    acc[0][t] = SWAP ? mma(cur[t], x0, acc[0][t]) : mma(x0, cur[t], acc[0][t]);
    if (PF) lo = trd(p0 + t * TS);
    SB();
    acc[1][t] = SWAP ? mma(cur[t], x1, acc[1][t]) : mma(x1, cur[t], acc[1][t]);
    if (PF) { hi = trd(p1 + t * TS); nxt[t] = cat(lo, hi); }
    SB();
  }
}
template <int TS> DI void read_w(v4u (&f)[5], lptr p0, lptr p1) {
#pragma unroll
  for (int t = 0; t < 5; ++t) f[t] = cat(trd(p0 + t * TS), trd(p1 + t * TS));
}
DI void zero_acc(f4 (&acc)[2][5]) {
#pragma unroll
  for (int j = 0; j < 2; ++j)
#pragma unroll
    for (int t = 0; t < 5; ++t) acc[j][t] = (f4){0.f, 0.f, 0.f, 0.f};
}
template <int N> DI void arrive(unsigned base, unsigned one) {
  asm volatile("s_mov_b64 exec, 1\n\tds_add_u32 %0, %1 offset:%2\n\ts_mov_b64 exec, -1" :: "v"(base), "v"(one), "n"(4 * N) : "memory");
}

template <int N, int NEED> DI void lds_wait(unsigned base, unsigned cv) {
  unsigned s, it;
  asm volatile(
      "s_nop 0\n\tv_readfirstlane_b32 %1, %0\n\ts_cmp_ge_u32 %1, %4\n\ts_cbranch_scc1 .Lw1_%=\n\t"
      "s_mov_b32 %2, 0x100000\n"
      ".Lw0_%=:\n\tds_read_b32 %0, %3 offset:%5\n\ts_waitcnt lgkmcnt(0)\n\tv_readfirstlane_b32 %1, %0\n\t"
      "s_cmp_ge_u32 %1, %4\n\ts_cbranch_scc1 .Lw1_%=\n\ts_sub_u32 %2, %2, 1\n\ts_cbranch_scc0 .Lw0_%=\n"
      ".Lw1_%=:"
      : "+v"(cv), "=&s"(s), "=&s"(it) : "v"(base), "n"(NEED), "n"(4 * N) : "scc", "memory");
}

__global__ __launch_bounds__(512, 2) void fused_kernel(Params prm) {
  __shared__ __attribute__((aligned(1024))) char smem_[163840];
  const lptr sm = (lptr)smem_;
  const unsigned sbase = (unsigned)(size_t)smem_;
  unsigned stamp[NST];
#pragma unroll
  for (int i = 0; i < NST; ++i) stamp[i] = 0;
  STAMP(0);
  const u64 cyc0 = __builtin_amdgcn_s_memtime();
  const int tid = __builtin_amdgcn_workitem_id_x();
  const int w = rfl(tid >> 6), m = w & 3, set = w >> 2;
  const int bid = __builtin_amdgcn_workgroup_id_x(), xcd = bid & 7, idx = bid >> 3;
  const int br = xcd >> 2, rl = idx >> 2, gl = idx & 3;
  const int r = ((xcd & 3) >> 1) * 8 + rl, g = (xcd & 1) * 4 + gl;
  const int q0 = (((rl + gl) % NROT) * 5) / NROT;
  const int mt = 2 * m + (set ^ 1);
  const int dom = (br * 16 + r) * 4 + m;
  const rsrc_t ws = __builtin_amdgcn_make_buffer_rsrc(prm.ws, 0, WS_END, 0x00020000);
  const v4i rx = mk_srd(br ? prm.xB : prm.xA, 2048 * 640 * 2);
  const v4i rw = mk_srd(br ? prm.wB : prm.wA, 640 * 1920 * 2);
  const v4i ro = mk_srd(br ? prm.oB : prm.oA, 640 * 640 * 2);
  unsigned xoff[4], woff[5];
  unsigned kc[5];
#pragma unroll
  for (int c = 0; c < 5; ++c) { int q = q0 + c; kc[c] = q >= 5 ? q - 5 : q; }
  const unsigned xstep = br ? 16 * 1280 : 32 * 1280;
  const unsigned sbx = sbase + m * 8192, sbw = sbase + m * 5120;
  auto issue_x = [&](auto C, auto DST) {
    constexpr int c = C, dst = DST;
    sfor<8>([&](auto I) { constexpr int i = I; dma<dst + i * 1024>(rx, xoff[i & 3], kc[c] * 256 + (i >> 2) * xstep, sbx); });
  };
  auto issue_w1 = [&](auto C, auto PART, auto DST, auto I) {
    constexpr int c = C, part = PART, dst = DST, i = I;
    dma<dst + i * 1024>(rw, woff[i], kc[c] * (128 * 3840) + part * 1280, sbw);
  };
  auto issue_w = [&](auto C, auto PART, auto DST) { sfor<5>([&](auto I) { issue_w1(C, PART, DST, I); }); };
  auto issue_o1 = [&](auto BB, auto I) {
    constexpr int b = BB, i = I;
    dma<b * SLOT + i * 1024>(ro, woff[i], b * 128 * 1280, sbw);
  };
  auto issue_o = [&](auto BB) { sfor<5>([&](auto I) { issue_o1(BB, I); }); };
  auto set_xoff = [&](int lg, int li) {
#pragma unroll
    for (int i = 0; i < 4; ++i) {
      const int rho = 4 * i + lg;
      const int grow = br ? (r * 128 + 32 * m + rho) : (r * 128 + 64 * (m >> 1) + 2 * rho + (m & 1));
      xoff[i] = grow * 1280 + ((li ^ rho) << 4);
    }
  };
  auto set_woff = [&](int l) {
#pragma unroll
    for (int i = 0; i < 5; ++i) {
      const int p = (5 * m + i) * 64 + l, rho = p / 10, c = p - rho * 10;
      woff[i] = (rho ^ (((rho >> 3) & 1) << 2)) * 3840 + c * 16 + g * 160;
    }
  };
  auto set_ooff = [&](int l) {
#pragma unroll
    for (int i = 0; i < 5; ++i) {
      const int p = (5 * m + i) * 64 + l, rho = p / 10, c = p - rho * 10;
      woff[i] = rho * 1280 + c * 16 + g * 160;
    }
  };
  v2u tag3, tag2 = {0u, 0u};
  v2u Oq[5];

  if (br == 0) {
    v2u Q[5];
    if (set == 0) {
      const int l = tid & 63, lg = l >> 4, li = l & 15, lq = (l >> 2) & 3, lp = l & 3;
      set_xoff(lg, li);
      const int pos = 4 * m + lg;
      const int hrow = r * 128 + (pos < 8 ? 2 * pos - 15 : 2 * pos - 32);
      const unsigned hoff = hrow < 0 ? OOB : hrow * 1280 + ((li ^ pos) << 4);
      auto issue_xa = [&](auto C) {
        constexpr int c = C, dst = A_X0 + (c & 1) * A_XS;
        issue_x(C, IC<dst>{});
        dma<dst + 32768>(rx, hoff, kc[c] * 256, sbase + m * 1024);
      };
      auto issue_wa = [&](auto N) { constexpr int n = N; issue_w(IC<n / 3>{}, IC<n % 3>{}, IC<(n & 3) * SLOT>{}); };
      issue_xa(IC<0>{});
      set_woff(l);
      issue_wa(IC<0>{});
      issue_wa(IC<1>{});
      tag3 = load_tag(prm.ws + WS_E3 + (dom << 7));
      const int wr0 = (8 * lg + 4 * (lg & 1) + lq) * 160 + lp * 8, wr1 = (8 * lg + 4 * ((lg & 1) ^ 1) + lq) * 160 + lp * 8;
      f4 hacc[2][2];
      hacc[0][0] = hacc[0][1] = hacc[1][0] = hacc[1][1] = (f4){0.f, 0.f, 0.f, 0.f};
      v4u xh[4];
      sfor<15>([&](auto J) {
        constexpr int j = J, c = j / 3, p = j % 3;
        wait_vm<(j == 14 ? 5 : (p == 2 ? 9 : 5))>();
        STAMP(16 + j);
        bar();
        if constexpr (j == 0) STAMP(1);
        if constexpr (p == 0) issue_wa(IC<j + 2>{});
        if constexpr (p == 1 && c < 4) issue_xa(IC<c + 1>{});
        if constexpr (p == 2 && c < 4) { issue_wa(IC<j + 1>{}); issue_wa(IC<j + 2>{}); }
        if constexpr (j == 13) { set_ooff(l); issue_o(IC<3>{}); }
        if constexpr (j == 14) {
#pragma unroll
          for (int t = 0; t < 5; ++t) Q[t] = ld8(opq(sm + A_VB + mt * 2560 + l * 8) + t * 512);
          issue_o(IC<0>{});
          bar();
        }
        if constexpr (p == 0) {
#pragma unroll
          for (int ks = 0; ks < 4; ++ks)
            xh[ks] = ld16(opq(sm + A_X0 + (c & 1) * A_XS + 32768 + li * 256 + (((4 * ks + lg) ^ li) << 4)));
        } else {
          const lptr b0 = opq(sm + (j & 3) * SLOT + wr0), b1 = opq(sm + (j & 3) * SLOT + wr1);
          const lptr c0 = opq(b0 + m * 32), c1 = opq(b1 + m * 32);
#pragma unroll
          for (int ks = 0; ks < 4; ++ks) {
            const v4u wa = cat(trd(c0 + ks * 5120), trd(c1 + ks * 5120));
            const v4u wb = cat(trd(b0 + ks * 5120 + 128), trd(b1 + ks * 5120 + 128));
            hacc[p - 1][0] = p == 1 ? mma(wa, xh[ks], hacc[p - 1][0]) : mma(xh[ks], wa, hacc[p - 1][0]);
            hacc[p - 1][1] = p == 1 ? mma(wb, xh[ks], hacc[p - 1][1]) : mma(xh[ks], wb, hacc[p - 1][1]);
          }
          if constexpr (j >= 13) {
            const lptr hb = opq(sm + (p == 1 ? A_KB : A_VB) + 40 * 512 + l * 8);
            st8(hb + m * 512, pk4(hacc[p - 1][0]));
            st8(hb + 4 * 512, pk4(hacc[p - 1][1]));
          }
        }
      });
    } else {
      const int l = tid & 63, lg = l >> 4, li = l & 15, lq = (l >> 2) & 3, lp = l & 3;
      tag3 = load_tag(prm.ws + WS_E3 + (dom << 7));
      const int wr0 = (8 * lg + 4 * (lg & 1) + lq) * 160 + lp * 8, wr1 = (8 * lg + 4 * ((lg & 1) ^ 1) + lq) * 160 + lp * 8;
      const int xrd = (32 * m + li) * 256;
      f4 acc[3][2][5];
      zero_acc(acc[0]); zero_acc(acc[1]); zero_acc(acc[2]);
      v4u xf[4][2], wf[2][5];
      auto read_x1 = [&](int c, int ks) {
        const lptr b = opq(sm + A_X0 + (c & 1) * A_XS + xrd + (((4 * ks + lg) ^ li) << 4));
        xf[ks][0] = ld16(b);
        xf[ks][1] = ld16(b + 4096);
      };
      barx();
      STAMP(1);
      read_x1(0, 0);
      read_w<32>(wf[0], opq(sm + wr0), opq(sm + wr1));
      sfor<15>([&](auto J) {
        constexpr int j = J, c = j / 3, p = j % 3;
        const lptr b0 = opq(sm + (j & 3) * SLOT + wr0), b1 = opq(sm + (j & 3) * SLOT + wr1);
        SB();
        sfor<3>([&](auto KS) {
          constexpr int ks = KS;
          if constexpr (p == 0) { read_x1(c, ks + 1); SB(); }
          kstep<(p < 2), true, 32>(wf[ks & 1], xf[ks][0], xf[ks][1], acc[p], wf[(ks + 1) & 1], b0 + (ks + 1) * 5120, b1 + (ks + 1) * 5120);
        });
        if constexpr (j < 14) {
          STAMP2(17 + j);
          if constexpr (j == 13) bar(); else barx();
          const lptr n0 = opq(sm + ((j + 1) & 3) * SLOT + wr0), n1 = opq(sm + ((j + 1) & 3) * SLOT + wr1);
          if constexpr (p == 2) read_x1(c + 1, 0);
          if constexpr (j == 13) {
#pragma unroll
            for (int t = 0; t < 5; ++t) Q[t] = ld8(opq(sm + A_VB + mt * 2560 + l * 8) + t * 512);
          }
          SB();
          kstep<(p < 2), true, 32>(wf[1], xf[3][0], xf[3][1], acc[p], wf[0], n0, n1);
        } else {
          kstep<false, false, 32>(wf[1], xf[3][0], xf[3][1], acc[p], wf[0], b0, b1);
        }
        if constexpr (j >= 12) {
          const lptr ob = opq(sm + (p == 1 ? A_KB : A_VB) + m * 5120 + l * 8);
#pragma unroll
          for (int jj = 0; jj < 2; ++jj)
#pragma unroll
            for (int t = 0; t < 5; ++t) st8(ob + (jj * 5 + t) * 512, pk4(acc[p][jj][t]));
          if constexpr (j == 13) bar();
          SB();
        }
      });
    }
    STAMP(2);
    LANE_VARS;
    const int pv = (mt & 1) ? mt - 1 : (m >= 2 ? mt - 3 : 8);
    v4u P[4];
    {
      v2u K[2][5];
      const lptr kc0 = opq(sm + A_KB + mt * 2560 + l * 8), kp0 = opq(sm + A_KB + pv * 2560 + l * 8);
#pragma unroll
      for (int t = 0; t < 5; ++t) { K[0][t] = ld8(kc0 + t * 512); K[1][t] = ld8(kp0 + t * 512); }
      f4 mc, mp;
      v2u selc;
      const bool halo = pv == 8, oddh = halo && (m & 1);
      const int plo = (halo && r == 0) ? 99 : (oddh ? li + 1 : li + 9), phi = oddh ? 7 : 15;
#pragma unroll
      for (int rr = 0; rr < 4; ++rr) {
        const int pos = 4 * lg + rr;
        mc[rr] = (pos <= li && pos >= li - 7) ? 0.f : -1e30f;
        mp[rr] = (pos >= plo && pos <= phi) ? 0.f : -1e30f;
      }
      selc[0] = (4 * lg <= li ? 0xffffu : 0u) | (4 * lg + 1 <= li ? 0xffff0000u : 0u);
      selc[1] = (4 * lg + 2 <= li ? 0xffffu : 0u) | (4 * lg + 3 <= li ? 0xffff0000u : 0u);
      constexpr float CS = 0.22360679774997896f * 1.4426950408889634f;
#pragma unroll
      for (int h = 0; h < 4; ++h) {
        const v2u z = {0u, 0u};
        const v4u qh = cat(lg >= h ? Q[h] : z, lg <= h ? Q[h + 1] : z);
        const f4 sc = mma(cat(K[0][h], K[0][h + 1]), qh, mc);
        const f4 sp = mma(cat(K[1][h], K[1][h + 1]), qh, mp);
        f4 s;
#pragma unroll
        for (int rr = 0; rr < 4; ++rr) s[rr] = __builtin_fmaxf(sc[rr], sp[rr]);
        float mx = max3(s[0], s[1], __builtin_fmaxf(s[2], s[3]));
        {
          auto a = __builtin_amdgcn_permlane32_swap(f2u(mx), f2u(mx), false, false);
          mx = __builtin_fmaxf(u2f(a[0]), u2f(a[1]));
          auto b = __builtin_amdgcn_permlane16_swap(f2u(mx), f2u(mx), false, false);
          mx = __builtin_fmaxf(u2f(b[0]), u2f(b[1]));
        }
        f4 e;
#pragma unroll
        for (int rr = 0; rr < 4; ++rr) e[rr] = ex2((s[rr] - mx) * CS);
        float sum = (e[0] + e[1]) + (e[2] + e[3]);
        {
          auto a = __builtin_amdgcn_permlane32_swap(f2u(sum), f2u(sum), false, false);
          sum = u2f(a[0]) + u2f(a[1]);
          auto b = __builtin_amdgcn_permlane16_swap(f2u(sum), f2u(sum), false, false);
          sum = u2f(b[0]) + u2f(b[1]);
        }
        const v2u pp = pk4(e * rcp(sum));
        P[h] = (v4u){pp[0] & selc[0], pp[1] & selc[1], pp[0] & ~selc[0], pp[1] & ~selc[1]};
      }
    }
    pin(P);
    STAMP(3);
    bar();
    const lptr vc0 = opq(sm + A_VB + mt * 2560 + l * 8), vp0 = opq(sm + A_VB + pv * 2560 + l * 8);
#pragma unroll
    for (int n = 0; n < 5; ++n) {
      const v4u vf = cat(ld8(vc0 + n * 512), ld8(vp0 + n * 512));
      const f4 zz = {0.f, 0.f, 0.f, 0.f};
      f4 o;
      if (n == 0) o = mma(vf, P[0], zz);
      else if (n == 4) o = mma(vf, P[3], zz);
      else {
        const f4 hi = mma(vf, P[n], zz), lo = mma(vf, P[n - 1], zz);
        o = lg >= n ? hi : lo;
      }
      Oq[n] = pk4(o);
    }
  } else {
    volatile LAS unsigned* cnt = (volatile LAS unsigned*)(sm + B_CNT);
    v2u Xq[5];
    if (set == 1) {
      v4u xr[5][4][2];
      int wr0, wr1;
      {
        const int l = tid & 63, lg = l >> 4, li = l & 15, lq = (l >> 2) & 3, lp = l & 3;
        set_xoff(lg, li);
        issue_x(IC<0>{}, IC<b_stage(0)>{});
        set_woff(l);
        issue_w(IC<0>{}, IC<1>{}, IC<b_stage(0) + 32768>{});
        issue_x(IC<1>{}, IC<b_stage(1)>{});
        tag3 = load_tag(prm.ws + WS_E3 + (dom << 7));
        wr0 = (8 * lg + 4 * (lg & 1) + lq) * 160 + lp * 40;
        wr1 = (8 * lg + 4 * ((lg & 1) ^ 1) + lq) * 160 + lp * 40;
        const int xrd = (32 * m + li) * 256;
        sfor<5>([&](auto C) {
          constexpr int c = C;
          wait_vm<(c < 4 ? 8 : 5)>();
          STAMP(16 + c);
          bar();
          if constexpr (c == 0) STAMP(1);
#pragma unroll
          for (int ks = 0; ks < 4; ++ks) {
            const lptr b = opq(sm + b_stage(c) + xrd + (((4 * ks + lg) ^ li) << 4));
            xr[c][ks][0] = ld16(b);
            xr[c][ks][1] = ld16(b + 4096);
          }
          if constexpr (c < 4) issue_w(IC<c + 1>{}, IC<1>{}, IC<b_stage(c + 1) + 32768>{});
          if constexpr (c < 3) issue_x(IC<c + 2>{}, IC<b_stage(c + 2)>{});
          if constexpr (c >= 3) issue_w(IC<c - 3>{}, IC<2>{}, IC<b_slot(c - 3)>{});
        });
      }
      wait_vm<5>();
      bar();
      STAMP(2);
      f4 acc[2][5];
      v4u wf[2][5];
      auto spin = [&](auto N, unsigned cv) {
        constexpr int n = N;
        lds_wait<n, ((n >= 2 && n <= 5) ? 8 : 4)>(sbase + B_CNT, cv);
      };
      sfor<10>([&](auto N) {
        constexpr int n = N;
        const lptr b0 = opq(sm + b_slot(n) + wr0), b1 = opq(sm + b_slot(n) + wr1);
        if constexpr (n == 5) {
          issue_w(IC<0>{}, IC<0>{}, IC<b_slot(5)>{});
          issue_w(IC<1>{}, IC<0>{}, IC<b_slot(6)>{});
          issue_w(IC<2>{}, IC<0>{}, IC<b_slot(7)>{});
          wait_vm<10>();
          arrive<5>(sbase + B_CNT, 1u);
          spin(IC<5>{}, 0u);
        }
        if constexpr (n % 5 == 0) {
          zero_acc(acc);
          read_w<8>(wf[0], b0, b1);
        }
        if constexpr (n == 7) set_ooff(lane_id());
        auto paced = [&](auto I) {
          if constexpr (n == 5 || n == 6) issue_w1(IC<n - 2>{}, IC<0>{}, IC<b_slot(n + 3)>{}, I);
          if constexpr (n >= 7) issue_o1(IC<n - 5>{}, I);
          SB();
        };
        constexpr bool more = n % 5 != 4;
        unsigned cv = 0;
        SB();
        paced(IC<0>{});
        kstep<true, true, 8>(wf[0], xr[n % 5][0][0], xr[n % 5][0][1], acc, wf[1], b0 + 5120, b1 + 5120);
        if constexpr (more && n > 0) {
          if constexpr (n >= 5) wait_vm<6>();
          arrive<n + 1>(sbase + B_CNT, 1u);
        }
        paced(IC<1>{});
        kstep<true, true, 8>(wf[1], xr[n % 5][1][0], xr[n % 5][1][1], acc, wf[0], b0 + 10240, b1 + 10240);
        if constexpr (more && n > 0) cv = cnt[n + 1];
        paced(IC<2>{});
        kstep<true, true, 8>(wf[0], xr[n % 5][2][0], xr[n % 5][2][1], acc, wf[1], b0 + 15360, b1 + 15360);
        if constexpr (more) {
          if constexpr (n == 0) {
            wait_vm<0>();
            arrive<1>(sbase + B_CNT, 1u);
          }
          spin(IC<n + 1>{}, cv);
          STAMP2(22 + n);
          const lptr n0 = opq(sm + b_slot(n + 1) + wr0), n1 = opq(sm + b_slot(n + 1) + wr1);
          paced(IC<3>{});
          kstep<true, true, 8>(wf[1], xr[n % 5][3][0], xr[n % 5][3][1], acc, wf[0], n0, n1);
        } else {
          paced(IC<3>{});
          kstep<true, false, 8>(wf[1], xr[n % 5][3][0], xr[n % 5][3][1], acc, wf[0], b0, b1);
        }
        paced(IC<4>{});
        if constexpr (n == 4) {
          LANE_VARS;
          const lptr vp = opq(sm + B_VIMG + lg * 6144 + (32 * m + li) * 48);
#pragma unroll
          for (int j = 0; j < 2; ++j) {
            st16(vp + j * 768, cat(pk4(acc[j][0]), pk4(acc[j][1])));
            st16(vp + j * 768 + 16, cat(pk4(acc[j][2]), pk4(acc[j][3])));
            st16(vp + j * 768 + 32, cat(pk4(acc[j][4]), (v2u){0x3f80u, 0u}));
          }
          STAMP(3);
          bar();
        }
      });
      const lptr qh = opq(sm + B_QH + m * 2560 + lane_id() * 8);
#pragma unroll
      for (int t = 0; t < 5; ++t) {
        Xq[t] = pk4(acc[0][t]);
        st8(qh + t * 512, pk4(acc[1][t]));
      }
      STAMP(4);
      bar();
    } else {
      const int l = tid & 63, lg = l >> 4, li = l & 15, lq = (l >> 2) & 3, lp = l & 3;
      {
        const v4i rp = mk_srd(prm.pB, 32 * 400 * 2);
        if (l < 50) dma<B_WPHI>(rp, l * 16, (g * 4 + m) * 800, sbase + m * 800);
      }
      tag3 = load_tag(prm.ws + WS_E3 + (dom << 7));
      tag2 = load_tag(prm.ws + WS_E2 + (g << 7));
      if (w == 0 && l < 16) cnt[l] = 0;
      set_woff(l);
      const int wr0 = (8 * lg + 4 * (lg & 1) + lq) * 160 + lp * 40, wr1 = (8 * lg + 4 * ((lg & 1) ^ 1) + lq) * 160 + lp * 40;
      const int xrd = (32 * m + li) * 256;
      f4 acc[2][5];
      zero_acc(acc);
      sfor<5>([&](auto C) {
        constexpr int c = C;
        if constexpr (c == 4) wait_vm<0>();
        STAMP(16 + c);
        bar();
        if constexpr (c == 0) STAMP(1);
        v4u xf[4][2], wf[2][5];
        const lptr b0 = opq(sm + b_stage(c) + 32768 + wr0), b1 = opq(sm + b_stage(c) + 32768 + wr1);
        auto rx1 = [&](int ks) {
          const lptr b = opq(sm + b_stage(c) + xrd + (((4 * ks + lg) ^ li) << 4));
          xf[ks][0] = ld16(b);
          xf[ks][1] = ld16(b + 4096);
        };
        auto paced = [&](auto I) {
          if constexpr (c == 4) issue_w1(IC<2>{}, IC<2>{}, IC<b_slot(2)>{}, I);
          SB();
        };
        rx1(0);
        read_w<8>(wf[0], b0, b1);
        rx1(1);
        paced(IC<0>{});
        kstep<true, true, 8>(wf[0], xf[0][0], xf[0][1], acc, wf[1], b0 + 5120, b1 + 5120);
        rx1(2);
        paced(IC<1>{});
        kstep<true, true, 8>(wf[1], xf[1][0], xf[1][1], acc, wf[0], b0 + 10240, b1 + 10240);
        rx1(3);
        paced(IC<2>{});
        kstep<true, true, 8>(wf[0], xf[2][0], xf[2][1], acc, wf[1], b0 + 15360, b1 + 15360);
        paced(IC<3>{});
        kstep<true, false, 8>(wf[1], xf[3][0], xf[3][1], acc, wf[0], b0, b1);
        paced(IC<4>{});
      });
      bar();
      STAMP(2);
      sfor<2>([&](auto JJ) {
        constexpr int j = JJ;
        v2u X[5], P[10];
#pragma unroll
        for (int t = 0; t < 5; ++t) X[t] = pk4(acc[j][t]);
        feature_map(X, opq(sm + B_WPHI + lg * 800 + lq * 40), P, [&](auto I) {
          constexpr int i = I;
          SB();
          if constexpr (j == 0) {
            if constexpr (i == 5) { wait_vm<5>(); arrive<2>(sbase + B_CNT, 1u); }
            issue_w1(IC<3 + i / 5>{}, IC<2>{}, IC<b_slot(3 + i / 5)>{}, IC<i % 5>{});
          } else {
            if constexpr (i == 0) { wait_vm<5>(); arrive<3>(sbase + B_CNT, 1u); }
            if constexpr (i == 4) { wait_vm<0>(); arrive<4>(sbase + B_CNT, 1u); }
          }
          SB();
        });
        const lptr rp = opq(sm + B_PHIIMG + lg * 10240 + (32 * m + 16 * j + li) * 80);
#pragma unroll
        for (int c = 0; c < 5; ++c) st16(rp + c * 16, cat(P[2 * c], P[2 * c + 1]));
      });
      STAMP(3);
      bar();
      STAMP(4);
      {
        f4 d0[3], d1[3];
#pragma unroll
        for (int b = 0; b < 3; ++b) d0[b] = d1[b] = (f4){0.f, 0.f, 0.f, 0.f};
        const lptr fp = opq(sm + B_PHIIMG + m * 10240 + (4 * lg + lq) * 80 + lp * 8);
        const lptr dp = opq(sm + B_VIMG + m * 6144 + (4 * lg + lq) * 48 + lp * 8);
#pragma unroll
        for (int ks = 0; ks < 4; ++ks) {
          v4u F[3], D[2];
#pragma unroll
          for (int a = 0; a < 2; ++a) D[a] = cat(trd(dp + ks * 1536 + a * 32), trd(dp + ks * 1536 + 768 + a * 32));
#pragma unroll
          for (int b = 0; b < 3; ++b) F[b] = cat(trd(fp + ks * 2560 + b * 32), trd(fp + ks * 2560 + 1280 + b * 32));
#pragma unroll
          for (int b = 0; b < 3; ++b) {
            d0[b] = mma(D[0], F[b], d0[b]);
            d1[b] = mma(D[1], F[b], d1[b]);
          }
        }
        arrive<5>(sbase + B_CNT, 1u);
        const unsigned so = WS_KP + (g * 4 + m) * 9 * 8192 + r * 512;
        const unsigned v0 = (l >> 5) * 8192 + (l & 31) * 16, v1 = l < 32 ? 2 * 8192 + l * 16 : OOB;
#pragma unroll
        for (int b = 0; b < 3; ++b) {
          gst16(ws, v0, so + b * 3 * 8192, __builtin_bit_cast(v4u, d0[b]));
          gst16(ws, v1, so + b * 3 * 8192, __builtin_bit_cast(v4u, d1[b]));
        }
        STAMP(5);
        publish_flag(ws, WS_F1 + (g * 4 + m) * 128 + r * 8, tag2, l);
        STAMP(6);
      }
      const int rid = m * 16 + r;
      if (rid < 36) {
        const int h = rid / 9;
        poll_flags(ws, WS_F1 + (g * 4 + h) * 128, l < 16 ? l * 8u : OOB, tag2);
        STAMP(7);
        f4 s = {0.f, 0.f, 0.f, 0.f};
        v4u pv[8];
#pragma unroll
        for (int i = 0; i < 8; ++i) pv[i] = gld16(ws, l * 16, WS_KP + (g * 36 + rid) * 8192 + i * 1024);
#pragma unroll
        for (int i = 0; i < 8; ++i) s += __builtin_bit_cast(f4, pv[i]);
#pragma unroll
        for (int e = 0; e < 4; ++e) {
          auto a = __builtin_amdgcn_permlane32_swap(f2u(s[e]), f2u(s[e]), false, false);
          s[e] = u2f(a[0]) + u2f(a[1]);
        }
        gst8(ws, l < 32 ? l * 8 : OOB, WS_KR + (g * 36 + rid) * 256, pk4(s));
        publish_flag(ws, WS_F2 + (g * 4 + h) * 128 + (rid - h * 9) * 8, tag2, l);
        STAMP(8);
      }
      bar();
#pragma unroll
      for (int t = 0; t < 5; ++t) Xq[t] = ld8(opq(sm + B_QH + m * 2560 + l * 8) + t * 512);
    }
    LANE_VARS;
    v2u Pq[10];
    feature_map(Xq, opq(sm + B_WPHI + lg * 800 + lq * 40), Pq, NoHook{});
    pin(Pq);
    STAMP(9);
    if (set == 0) {
      poll_flags(ws, WS_F2 + (g * 4 + m) * 128, l < 9 ? l * 8u : OOB, tag2);
      v4u kr[3];
#pragma unroll
      for (int i = 0; i < 3; ++i)
        kr[i] = gld16(ws, (i < 2 || l < 16) ? l * 16 : OOB, WS_KR + (g * 4 + m) * 2304 + i * 1024);
      const lptr rw_ = opq(sm + B_R + m * 2304 + l * 16);
      st16(rw_, kr[0]);
      st16(rw_ + 1024, kr[1]);
      if (l < 16) st16(rw_ + 2048, kr[2]);
    } else {
      issue_o(IC<1>{});
      issue_o(IC<0>{});
    }
    bar();
    STAMP(10);
    if (w == 0 && r == 0) gst8(ws, l == 0 ? 0u : OOB, WS_E2 + (g << 7), tag2);
    f4 nm[6];
#pragma unroll
    for (int tt = 0; tt < 6; ++tt) nm[tt] = (f4){0.f, 0.f, 0.f, 0.f};
    const lptr r_rd = opq(sm + B_R + lg * 2304 + lq * 8);
#pragma unroll
    for (int u = 0; u < 10; ++u)
#pragma unroll
      for (int tt = 0; tt < 6; ++tt) nm[tt] = mma4(trd(r_rd + (u >> 2) * 768 + tt * 128 + (u & 3) * 32), Pq[u], nm[tt]);
    const float id = rcp(nm[5][0] + 1e-6f);
#pragma unroll
    for (int t = 0; t < 5; ++t) Oq[t] = pk4(nm[t] * id);
  }

  pin(Oq);
  STAMP(11);
  LANE_VARS;
  {
    const unsigned so = WS_O + ((dom * 2 + (set ^ 1)) * 8 + g) * 2560;
    gst16(ws, l * 16, so, cat(Oq[0], Oq[1]));
    gst16(ws, l * 16, so + 1024, cat(Oq[2], Oq[3]));
    gst8(ws, l * 8, so + 2048, Oq[4]);
    if (br == 0 && set == 0) {
      issue_o(IC<4>{});
      wait_vm<5>();
      gst8(ws, l == 0 ? 0u : OOB, WS_F3 + (dom << 7) + 64 + g * 8, tag3);
      issue_o(IC<1>{});
      issue_o(IC<2>{});
      wait_vm<0>();
    } else {
      publish_flag(ws, WS_F3 + (dom << 7) + (set ^ 1) * 64 + g * 8, tag3, l);
    }
  }
  STAMP(12);
  bar();
  f4 acc[2][5];
  zero_acc(acc);
  auto gemm2 = [&](auto BRC) {
    constexpr int TS = BRC ? 4 : 16;
    const lptr wo_rd = opq(sm + set * (320 * 160) + ((BRC ? 20 : 4) * lg + lq) * 160 + lp * 40);
    auto r0 = [](int s) { const int k = s % 5; return 160 * (s / 5) + (k < 4 ? 80 * (k >> 1) + TS * 2 * (k & 1) : TS * 4); };
    auto r1 = [&](int s) { return r0(s) + (s % 5 < 4 ? TS : 80); };
    v4u wf[2][5];
    read_w<8>(wf[0], wo_rd + r0(0) * 160, wo_rd + r1(0) * 160);
    poll_flags(ws, WS_F3 + (dom << 7) + set * 32, l < 8 ? (l >> 2) * 64u + (l & 3) * 8u : OOB, tag3);
    STAMP(13);
    v4u of[10][2];
#pragma unroll
    for (int s = 0; s < 10; ++s)
#pragma unroll
      for (int jj = 0; jj < 2; ++jj) {
        const unsigned so = WS_O + ((dom * 2 + jj) * 8 + 4 * set + 2 * (s / 5)) * 2560;
        const int k = s % 5;
        of[s][jj] = k < 4 ? gld16(ws, l * 16, so + (k >> 1) * 2560 + (k & 1) * 1024)
                          : cat(gld8(ws, l * 8, so + 2048), gld8(ws, l * 8, so + 2560 + 2048));
      }
    SB();
    sfor<10>([&](auto S) {
      constexpr int s = S, sn = s < 9 ? s + 1 : s;
      kstep<true, (s < 9), 8>(wf[s & 1], of[s][0], of[s][1], acc, wf[(s + 1) & 1], wo_rd + r0(sn) * 160, wo_rd + r1(sn) * 160);
    });
  };
  if (br) gemm2(IC<1>{});
  else gemm2(IC<0>{});
  pin(acc[0]);
  pin(acc[1]);
  STAMP(14);
  f4 res[5];
  {
    const lptr xw = opq(sm + XCH + w * 5120 + l * 16), xr_ = opq(sm + XCH + (w ^ 4) * 5120 + l * 16);
#pragma unroll
    for (int t = 0; t < 5; ++t) {
      st16(xw + t * 1024, __builtin_bit_cast(v4u, set ? acc[1][t] : acc[0][t]));
      res[t] = set ? acc[0][t] : acc[1][t];
    }
    bar();
    if (g == 0 && set == 0) gst8(ws, l == 0 ? 0u : OOB, WS_E3 + (dom << 7), tag3);
#pragma unroll
    for (int t = 0; t < 5; ++t) res[t] += __builtin_bit_cast(f4, ld16(xr_ + t * 1024));
  }
  {
    const int jj = mt & 1;
    const int srow = br ? (r * 128 + 16 * mt + li) : (r * 128 + 64 * (m >> 1) + 2 * (16 * jj + li) + (m & 1));
    const rsrc_t rout = __builtin_amdgcn_make_buffer_rsrc(prm.out, 0, 2048 * 1280 * 2, 0x00020000);
    const unsigned vo = srow * 2560 + lg * 40, so = br * 1280 + g * 160;
    __builtin_amdgcn_raw_buffer_store_b128(cat(pk4(res[0]), pk4(res[1])), rout, vo, so, 17);
    __builtin_amdgcn_raw_buffer_store_b128(cat(pk4(res[2]), pk4(res[3])), rout, vo, so + 16, 17);
    __builtin_amdgcn_raw_buffer_store_b64(pk4(res[4]), rout, vo, so + 32, 17);
  }
  wait_vm<0>();
  STAMP(15);
  {
    const unsigned tgt = 6000u + ((false) ? (stamp[0] - stamp[0]) * 1u : 0u);
    for (int it = 0; it < 4000000; ++it) {
      if ((unsigned)__builtin_amdgcn_s_memrealtime() - stamp[0] >= tgt) break;
      __builtin_amdgcn_s_sleep(2);
    }
  }
}

#if defined(__HIP_DEVICE_COMPILE__)
#pragma clang attribute pop
#endif

extern "C" void kernel_launch(void* const* d_in, const int* in_sizes, int n_in, void* d_out, int out_size, void* d_ws,
                              size_t ws_size, hipStream_t stream) {
  Params p;
  p.xA = (const u16*)d_in[0]; p.wA = (const u16*)d_in[1]; p.oA = (const u16*)d_in[2];
  p.xB = (const u16*)d_in[3]; p.wB = (const u16*)d_in[4]; p.pB = (const u16*)d_in[5]; p.oB = (const u16*)d_in[6];
  p.out = (u16*)d_out; p.ws = (char*)d_ws;
  fused_kernel<<<256, 512, 0, stream>>>(p);
}
```

```cpp
#include <hip/hip_runtime.h>
#include <hip/hip_bf16.h>
#include <type_traits>

#define PROFILE 0
#define NROT 4

#define NOPK 1
#if defined(__HIP_DEVICE_COMPILE__) && NOPK
#pragma clang attribute push(__attribute__((target("no-packed-fp32-ops"))), apply_to = function)
#endif
typedef unsigned short u16;
typedef unsigned long long u64;
typedef unsigned v2u __attribute__((ext_vector_type(2)));
typedef unsigned v4u __attribute__((ext_vector_type(4)));
typedef int v4i __attribute__((ext_vector_type(4)));
typedef float f2 __attribute__((ext_vector_type(2)));
typedef float f4 __attribute__((ext_vector_type(4)));
typedef short s4 __attribute__((ext_vector_type(4)));
typedef __bf16 bf2 __attribute__((ext_vector_type(2)));
typedef __bf16 bf8 __attribute__((ext_vector_type(8)));
#define LAS __attribute__((address_space(3)))
#define GAS __attribute__((address_space(1)))
#define DI __device__ __forceinline__
typedef LAS char* lptr;

constexpr unsigned WS_O = 4096;
constexpr unsigned WS_KP = 5246976;
constexpr unsigned WS_KR = WS_KP + 8 * 32 * 4096;
constexpr unsigned WS_F3 = WS_KR + 8 * 8192;
constexpr unsigned WS_E3 = WS_F3 + 512 * 128;
constexpr unsigned WS_F1 = WS_E3 + 32 * 128;
constexpr unsigned WS_E2 = WS_F1 + 8 * 2048;
constexpr unsigned WS_END = WS_E2 + 8 * 128;
constexpr unsigned WS_DBG = 16u << 20;
constexpr unsigned OOB = 0x7f000000u;
constexpr u64 TAG_STEP = 0x9E3779B97F4A7C15ull;
constexpr unsigned SPIN_CAP = 1u << 16;

template <int N> using IC = std::integral_constant<int, N>;
template <int I, int N, class F> DI void sfor_(F&& f) {
  if constexpr (I < N) { f(IC<I>{}); sfor_<I + 1, N>(f); }
}
template <int N, class F> DI void sfor(F&& f) { sfor_<0, N>(f); }

DI unsigned f2u(float x) { return __builtin_bit_cast(unsigned, x); }
DI float u2f(unsigned x) { return __builtin_bit_cast(float, x); }
DI unsigned rfl(unsigned x) { return __builtin_amdgcn_readfirstlane(x); }
DI v4i mk_srd(const void* p, unsigned bytes) {
  u64 a = (u64)p;
  v4i r;
  r.x = rfl((unsigned)a); r.y = rfl((unsigned)(a >> 32) & 0xffffu); r.z = bytes; r.w = 0x00020000;
  return r;
}
template <int IMM, bool NT = false> DI void dma(v4i rs, unsigned voff, unsigned soff, unsigned sbase) {
  if constexpr (NT)
    asm volatile("s_add_u32 m0, %3, %4\n\ts_nop 0\n\tbuffer_load_dwordx4 %0, %1, %2 offen nt lds"
                 :: "v"(voff), "s"(rs), "s"(soff), "s"(sbase), "n"(IMM) : "memory", "scc");
  else
    asm volatile("s_add_u32 m0, %3, %4\n\ts_nop 0\n\tbuffer_load_dwordx4 %0, %1, %2 offen lds"
                 :: "v"(voff), "s"(rs), "s"(soff), "s"(sbase), "n"(IMM) : "memory", "scc");
}
template <int IMM> DI void dma_lo(v4i rs, unsigned voff, unsigned soff, unsigned sbase) {
  asm volatile("s_add_u32 m0, %3, %4\n\ts_mov_b32 exec_hi, 0\n\tbuffer_load_dwordx4 %0, %1, %2 offen lds\n\ts_mov_b32 exec_hi, -1"
               :: "v"(voff), "s"(rs), "s"(soff), "s"(sbase), "n"(IMM) : "memory", "scc");
}
template <int N> DI void wait_vm() { asm volatile("s_waitcnt vmcnt(%0)" :: "n"(N) : "memory"); }
DI void bar() { asm volatile("s_waitcnt lgkmcnt(0)\n\ts_barrier" ::: "memory"); }
DI void barx() { asm volatile("s_barrier" ::: "memory"); }
#define SB() __builtin_amdgcn_sched_barrier(0)
DI unsigned pk2(float a, float b) { return __builtin_bit_cast(unsigned, __builtin_convertvector((f2){a, b}, bf2)); }
DI v2u pk4(f4 v) { return (v2u){pk2(v[0], v[1]), pk2(v[2], v[3])}; }
DI v4u cat(v2u a, v2u b) { return (v4u){a[0], a[1], b[0], b[1]}; }
DI f4 mma(v4u a, v4u b, f4 c) {
  return __builtin_amdgcn_mfma_f32_16x16x32_bf16(__builtin_bit_cast(bf8, a), __builtin_bit_cast(bf8, b), c, 0, 0, 0);
}
template <int AB> DI f4 mma4(v2u a, v2u b, f4 c) {
  return __builtin_amdgcn_mfma_f32_4x4x4bf16_1k(__builtin_bit_cast(s4, a), __builtin_bit_cast(s4, b), c, 2, AB, 0);
}
DI v2u trd(lptr p) { return __builtin_bit_cast(v2u, __builtin_amdgcn_ds_read_tr16_b64_v4i16((LAS s4*)p)); }
DI lptr opq(lptr p) { unsigned a = (unsigned)(size_t)p; asm volatile("" : "+v"(a)); return (lptr)(size_t)a; }
DI v4u ld16(lptr p) { return *(LAS v4u*)p; }
DI v2u ld8(lptr p) { return *(LAS v2u*)p; }
DI void st16(lptr p, v4u v) { *(LAS v4u*)p = v; }
DI void st8(lptr p, v2u v) { *(LAS v2u*)p = v; }
DI float ex2(float x) { return __builtin_amdgcn_exp2f(x); }
DI float rcp(float x) { return __builtin_amdgcn_rcpf(x); }
DI float max3(float a, float b, float c) { return __builtin_fmaxf(__builtin_fmaxf(a, b), c); }
DI float min3(float a, float b, float c) { return __builtin_fminf(__builtin_fminf(a, b), c); }
constexpr int SC1 = 16;
typedef __amdgpu_buffer_rsrc_t rsrc_t;
DI v4u gld16(rsrc_t r, unsigned v, unsigned s) { return __builtin_amdgcn_raw_buffer_load_b128(r, v, s, SC1); }
DI v2u gld8(rsrc_t r, unsigned v, unsigned s) { return __builtin_amdgcn_raw_buffer_load_b64(r, v, s, SC1); }
template <int SC = 1> DI void gst16(rsrc_t r, unsigned v, unsigned s, v4u d) {
  if constexpr (SC == 1) asm volatile("buffer_store_dwordx4 %0, %1, %2, %3 offen sc1\n\ts_nop 1" :: "v"(d), "v"(v), "s"(r), "s"(s) : "memory");
  else if constexpr (SC == 2) asm volatile("buffer_store_dwordx4 %0, %1, %2, %3 offen nt\n\ts_nop 1" :: "v"(d), "v"(v), "s"(r), "s"(s) : "memory");
  else asm volatile("buffer_store_dwordx4 %0, %1, %2, %3 offen\n\ts_nop 1" :: "v"(d), "v"(v), "s"(r), "s"(s) : "memory");
}
DI void gst8(rsrc_t r, unsigned v, unsigned s, v2u d) { __builtin_amdgcn_raw_buffer_store_b64(d, r, v, s, SC1); }
DI int lane_id() {
  int x;
  asm volatile("v_mbcnt_lo_u32_b32 %0, -1, 0\n\tv_mbcnt_hi_u32_b32 %0, -1, %0" : "=v"(x));
  return x;
}
DI v2u next_tag(v2u e) {
  const u64 t = (((u64)e[1] << 32) | e[0]) + TAG_STEP;
  return (v2u){(unsigned)t, (unsigned)(t >> 32)};
}
#define LANE_VARS const int l = lane_id(), lg = l >> 4, li = l & 15, lq = (l >> 2) & 3, lp = l & 3; (void)lg; (void)li; (void)lq; (void)lp
DI void poll_flags(rsrc_t ws, unsigned soff, unsigned vo, v2u tag) {
#pragma unroll 1
  for (unsigned it = 0; it < SPIN_CAP; ++it) {
    asm volatile("" ::: "memory");
    v2u f = gld8(ws, vo, soff);
    bool ok = (vo == OOB) || (f[0] == tag[0] && f[1] == tag[1]);
    if (!__builtin_amdgcn_ballot_w64(!ok)) break;
    __builtin_amdgcn_s_sleep(1);
  }
  asm volatile("" ::: "memory");
}
DI void publish_flag(rsrc_t ws, unsigned soff, v2u tag, int l) {
  wait_vm<0>();
  gst8(ws, l == 0 ? 0u : OOB, soff, tag);
}

#define STAMP3(i)
#define STAMP(i)
#define STAMP2(i)

struct Params {
  const u16 *xA, *wA, *oA, *pA, *xB, *wB, *oB, *pB;
  u16* out;
  char* ws;
};
typedef unsigned v8u __attribute__((ext_vector_type(8)));
DI v4i srd(unsigned lo, unsigned hi, unsigned bytes) { return (v4i){(int)lo, (int)(hi & 0xffffu), (int)bytes, 0x00020000}; }
DI constexpr u64 rot_lut() {
  u64 v = 0;
  for (int i = 0; i < 11; ++i) v |= (u64)(((i % NROT) * 20 + NROT) / (2 * NROT)) << (4 * i);
  return v;
}

constexpr int BLK = 20480;
constexpr int STG_W = 18432;
constexpr int STG = 38912;
DI constexpr int slot_of(int s) { return ((12 - s) & 3) * STG; }
#define LAG 9
#define LAGE 9
#define LAGZ 9
#define NEARLY 3
#define NTW 0
#define FW 1
#define LPR 4
#define PRE0 0
#define PRE1B 40
#define E0L 1
#define APR 2
#define ADELAY 28
#define NTN 0
DI constexpr int bar_pos(int s, int nps) {
  const int lim = s <= 7 ? (s + 3) * nps : 10 * nps + (s == 8 ? 5 : 15);
  const int want = (s + 1) * nps + (s < NEARLY ? LAGE : (s >= 9 ? LAGZ : LAG));
  return want < lim ? want : lim;
}
DI constexpr int wq_at(int b) { return (4 - b) * BLK; }
constexpr int CNT = 163584, JUNK = 161024;
constexpr int A_KB = 102400, A_VB = 125440, A_QH = 148480, A_HP = A_QH, A_WO = 0, A_OUT = 102400;
constexpr int B_VIMG = 0, B_PHI = 102400, B_R = 122880, B_RS = 2688, B_QH = 143360, B_WPHI = 157696, B_WO = BLK, B_OUT = 133632;
constexpr int C_PROG = 0, C_KV = 8, C_HP = 9, C_V = 10, C_FL = 11, C_Q = 12, C_ACK = 16, C_GO = 18, C_WO = 20, C_R = 21;
#define PF 0
#define GPRIO 1
#define PSTG 0
#define OEARLY 1
#define GPRIO1 0
#define TPRIO 1
#define WOT 1
#define RDN 11
#define OSC 2
#define SPLIT 1
#define B10L 0
#define RING 9
#define PRE1 30
#define TLBW 0
#define WSLP 3
#define WQH 15
#define KRPOS 1
#define CFE 0
#define OSLP 4
#define RSLP 8
#define AHEAD 5
constexpr unsigned POISON = 0xffffffffu;

template <class T, int N> DI void pin(T (&a)[N]) {
#pragma unroll
  for (int i = 0; i < N; ++i) asm volatile("" : "+v"(a[i]));
}
DI void lds_bar() {
  asm volatile("" ::: "memory");
  __builtin_amdgcn_s_waitcnt(0xc07f);
  asm volatile("s_barrier" ::: "memory");
}

DI void load_wphi(v2u (&F)[7], lptr p, lptr q) {
#pragma unroll
  for (int t = 0; t < 5; ++t) F[t] = trd(p + t * 160);
  F[5] = trd(q + 32);
  F[6] = trd(p + 4 * 160 + 32);
}
typedef unsigned v10u __attribute__((ext_vector_type(10)));
template <class P> DI constexpr u64 lane_mask(P p) {
  u64 v = 0;
  for (int l = 0; l < 64; ++l) v |= (u64)(p(l) ? 1 : 0) << l;
  return v;
}
#define LANES(m) __builtin_amdgcn_inverse_ballot_w64(m)
DI float vmax3(float a, float b, float c) { float d; asm("v_max3_f32 %0, %1, %2, %3" : "=v"(d) : "v"(a), "v"(b), "v"(c)); return d; }
DI float vmin3(float a, float b, float c) { float d; asm("v_min3_f32 %0, %1, %2, %3" : "=v"(d) : "v"(a), "v"(b), "v"(c)); return d; }
DI float vamax3(float a, float b, float c) { float d; asm("v_max3_f32 %0, |%1|, |%2|, |%3|" : "=v"(d) : "v"(a), "v"(b), "v"(c)); return d; }
template <class H> DI void fm_exp(const v2u (&X)[5], const v2u (&F)[7], f4 (&e)[10], H&& hook) {
  f4 p[5];
  sfor<25>([&](auto N) {
    constexpr int n = N, t = n / 5, u = n % 5;
    p[u] = mma4<(u < 4 ? u : (t & 3))>(F[u < 4 ? t : (t < 4 ? 5 : 6)], X[t], t ? p[u] : (f4){0.f, 0.f, 0.f, 0.f});
    SB();
  });
  hook(IC<0>{});
  asm volatile("s_nop 4" : "+v"(p[0]), "+v"(p[1]), "+v"(p[2]), "+v"(p[3]), "+v"(p[4]));
  SB();
#pragma unroll
  for (int u = 0; u < 5; ++u) p[u] *= 1.4426950408889634f;
  SB();
  float a[6];
#pragma unroll
  for (int u = 0; u < 5; ++u) a[u] = vamax3(p[u][0], p[u][1], p[u][2]);
  a[5] = vamax3(p[0][3], p[1][3], p[2][3]);
  SB();
  a[0] = vamax3(a[0], a[1], p[3][3]);
  a[2] = vamax3(a[2], a[3], p[4][3]);
  a[4] = vamax3(a[4], a[5], a[0]);
  SB();
  if (__builtin_expect(__builtin_amdgcn_ballot_w64(!(vamax3(a[4], a[2], a[2]) < 40.f)) == 0, 1)) {
#pragma unroll
    for (int u = 0; u < 5; ++u)
#pragma unroll
      for (int i = 0; i < 4; ++i) { e[u][i] = ex2(p[u][i]); e[5 + u][i] = ex2(-p[u][i]); }
  } else {
    float mx = p[0][0], mn = p[0][0];
#pragma unroll
    for (int u = 0; u < 5; ++u) {
      mx = vmax3(vmax3(mx, p[u][0], p[u][1]), p[u][2], p[u][3]);
      mn = vmin3(vmin3(mn, p[u][0], p[u][1]), p[u][2], p[u][3]);
    }
#pragma unroll
    for (int u = 0; u < 5; ++u)
#pragma unroll
      for (int i = 0; i < 4; ++i) { e[u][i] = ex2(p[u][i] - mx); e[5 + u][i] = ex2(mn - p[u][i]); }
  }
  SB();
  hook(IC<1>{});
  SB();
}
DI float sum20(const f4* e) {
  const f4 a = ((e[0] + e[1]) + (e[2] + e[3])) + e[4];
  return (a[0] + a[1]) + (a[2] + a[3]);
}

template <bool SWAP, bool FIRST> DI void mmat(f4& c, v4u w, v4u x) {
  const v4u a = SWAP ? w : x, b = SWAP ? x : w;
  if constexpr (FIRST) asm volatile("v_mfma_f32_16x16x32_bf16 %0, %1, %2, 0" : "=&v"(c) : "v"(a), "v"(b));
  else asm volatile("v_mfma_f32_16x16x32_bf16 %0, %1, %2, %0" : "+v"(c) : "v"(a), "v"(b));
}
DI void settle(f4 (&a)[5]) { asm volatile("s_nop 7" : "+v"(a[0]), "+v"(a[1]), "+v"(a[2]), "+v"(a[3]), "+v"(a[4])); }
template <bool PFN, int TS, bool FIRST>
DI void kstep(const v4u (&cur)[5], v4u x0, v4u x1, f4 (&acc)[2][5], v4u (&nxt)[5], lptr p0) {
#pragma unroll
  for (int t = 0; t < 5; ++t) {
    v2u lo, hi;
    mmat<true, FIRST>(acc[0][t], cur[t], x0);
    if (PFN) lo = trd(p0 + t * TS);
    SB();
    mmat<true, FIRST>(acc[1][t], cur[t], x1);
    if (PFN) { hi = trd(p0 + 2560 + t * TS); nxt[t] = cat(lo, hi); }
    SB();
  }
}
struct Frag { v4u x[5], w[5]; };
template <int NX, int TS, int I> DI void frag_read(Frag& f, v2u& lo, lptr px, lptr ph, lptr pw) {
  if constexpr (I < 4) f.x[I] = ld16(px + I * 2048);
  else if constexpr (I < NX) f.x[4] = ld16(ph);
  else if constexpr (I < NX + 10) {
    constexpr int t = (I - NX) >> 1;
    if constexpr (((I - NX) & 1) == 0) lo = trd(pw + t * TS);
    else f.w[t] = cat(lo, trd(pw + 2560 + t * TS));
  }
}
DI void bump(unsigned& a, unsigned d) { asm volatile("v_add_u32 %0, %1, %0" : "+v"(a) : "s"(d)); }
template <int NXC, int NXN, int TS>
DI void kstep4(const Frag& cur, Frag& nxt, f4 (&acc)[5][5], unsigned& px, unsigned& ph, unsigned& pw, unsigned d) {
  v2u lo;
  sfor<5 * NXC>([&](auto N) {
    constexpr int n = N, t = n / NXC, j = n % NXC;
    mmat<true, false>(acc[j][t], cur.w[t], cur.x[j]);
    if constexpr (NXN > 0) {
      frag_read<NXN, TS, n>(nxt, lo, (lptr)(size_t)px, (lptr)(size_t)ph, (lptr)(size_t)pw);
      if constexpr (n == 15) bump(px, d);
      if constexpr (n == 16) bump(pw, d);
      if constexpr (n == 17 && NXN == 5) bump(ph, d);
    }
    SB();
  });
}
template <int TS> DI void read_w(v4u (&f)[5], lptr p0, lptr p1) {
#pragma unroll
  for (int t = 0; t < 5; ++t) f[t] = cat(trd(p0 + t * TS), trd(p1 + t * TS));
}
template <int N> DI void arrive(unsigned base, unsigned one) {
  asm volatile("s_mov_b64 exec, 1\n\tds_add_u32 %0, %1 offset:%2\n\ts_mov_b64 exec, -1" :: "v"(base), "v"(one), "n"(4 * N) : "memory");
}
template <int N, int NEED, int SLP = 1> DI void lds_wait(unsigned base, unsigned cv) {
  unsigned s, it;
  asm volatile(
      "s_nop 0\n\tv_readfirstlane_b32 %1, %0\n\ts_cmp_ge_u32 %1, %4\n\ts_cbranch_scc1 .Lw1_%=\n\t"
      "s_mov_b32 %2, 0x100000\n"
      ".Lw0_%=:\n\tds_read_b32 %0, %3 offset:%5\n\ts_waitcnt lgkmcnt(0)\n\tv_readfirstlane_b32 %1, %0\n\t"
      "s_cmp_ge_u32 %1, %4\n\ts_cbranch_scc1 .Lw1_%=\n\t"
      "s_sleep %6\n\t"
      "s_sub_u32 %2, %2, 1\n\ts_cbranch_scc0 .Lw0_%=\n"
      ".Lw1_%=:"
      : "+v"(cv), "=&s"(s), "=&s"(it) : "v"(base), "n"(NEED), "n"(4 * N), "n"(SLP) : "scc", "memory");
}
DI unsigned unp(unsigned x) { return x < POISON ? x : POISON - 1; }
DI v2u unp(v2u x) { return (v2u){unp(x[0]), unp(x[1])}; }
DI v4u unp(v4u x) { return (v4u){unp(x[0]), unp(x[1]), unp(x[2]), unp(x[3])}; }
DI unsigned umax(unsigned a, unsigned b) { return a > b ? a : b; }
DI unsigned mx2(unsigned a, v2u x) { return umax(umax(a, x[0]), x[1]); }
DI unsigned mx4(unsigned a, v4u x) { return umax(umax(umax(umax(a, x[0]), x[1]), x[2]), x[3]); }
DI bool any_poison(unsigned mxv) { return __builtin_amdgcn_ballot_w64(mxv == POISON) != 0; }
DI void check_flags(rsrc_t ws, unsigned soff, unsigned vo, v2u tag, v2u f) {
  const bool ok = (vo == OOB) || (f[0] == tag[0] && f[1] == tag[1]);
  if (__builtin_amdgcn_ballot_w64(!ok)) poll_flags(ws, soff, vo, tag);
}
DI void touch(v4i rs, unsigned voff, unsigned soff, unsigned junk) {
  asm volatile("s_mov_b32 m0, %3\n\ts_nop 0\n\tbuffer_load_dword %0, %1, %2 offen lds" :: "v"(voff), "s"(rs), "s"(soff), "s"(junk) : "memory");
}
DI constexpr bool step_tail(int s) { return s == 19 || (s >= 2 && (s - 2) % 5 == 4); }
DI constexpr int step_e(int s) { return s < 2 ? s : s - (s - 2) / 5; }
DI lptr ladd(unsigned v, unsigned s) {
  unsigned a;
  asm volatile("v_add_u32 %0, %1, %2" : "=v"(a) : "s"(s), "v"(v));
  return (lptr)(size_t)a;
}
DI void set_woff(unsigned (&woff)[5], int l, bool perm, unsigned stride) {
#pragma unroll
  for (int i = 0; i < 5; ++i) {
    const unsigned p = i * 64 + l, rho = (p * 6554u) >> 16, c = p - rho * 10;
    woff[i] = (perm ? ((rho & 3) | ((rho & 12) << 1) | ((rho & 16) >> 2)) : rho) * stride + c * 16;
  }
}

__global__ __launch_bounds__(512, 2) void fused_kernel(Params prm) {
  __shared__ __attribute__((aligned(1024))) char smem_[163840];
  const lptr sm = (lptr)smem_;
  const unsigned sbase = (unsigned)(size_t)smem_;
  const int bid = __builtin_amdgcn_workgroup_id_x(), xcd = bid & 7, idx = bid >> 3;
  const int br = xcd >> 2, rl = idx >> 3, g = idx & 7;
  v8u ka;
  v4u kb;
  u64 pc0;
  asm volatile("s_load_dwordx8 %0, %3, %4\n\ts_load_dwordx4 %1, %3, 0x40\n\ts_getpc_b64 %2\n.Lfk_top:"
               : "=&s"(ka), "=&s"(kb), "=&s"(pc0) : "s"(__builtin_amdgcn_kernarg_segment_ptr()), "s"(br * 32));
  SB();
  STAMP(0);
  const int tid = __builtin_amdgcn_workitem_id_x();
  const int w = rfl(tid >> 6), m = w & 3, set = w >> 2;
  const int r = (xcd & 3) * 4 + rl;
  const unsigned q0 = (unsigned)(rot_lut() >> (4 * (rl + g))) & 15u;
  const unsigned sbw = sbase + m * 5120, sbx = sbase + m * 4096, sbh = sbase + m * 512;
  unsigned woff[5], xv[4];
  const unsigned xs = (r * 128 + 64 * (m >> 1) + ((m & 1) << (5 * br))) * 1280;
  const unsigned wsb = g * 160 + (m & 1) * (32 * 3840) + (1 + (m >> 1)) * 1280;
  if (set == 0) {
    const int l = tid & 63;
    const unsigned rs = 2560 >> br;
#pragma unroll
    for (int i = 0; i < 4; ++i) xv[i] = (8 * i + (l >> 3)) * rs + (((l & 7) ^ (l >> 4) ^ (4 * (i & 1))) << 4);
    set_woff(woff, l, true, 3840);
    pin(xv); pin(woff);
  } else if (m == 0) {
    int len;
    asm volatile("s_mov_b32 %0, .Lfk_end0-.Lfk_top\n\ts_max_i32 %0, %0, .Lfk_end1-.Lfk_top\n\ts_max_i32 %0, %0, 0" : "=s"(len) :: "scc");
    touch(srd((unsigned)pc0, (unsigned)(pc0 >> 32), len), ((tid & 63) * 32 + idx) * 128, 0, sbase + JUNK);
  }
  asm volatile("s_waitcnt lgkmcnt(0)" : "+s"(ka), "+s"(kb));
  const v4i rx = srd(ka[0], ka[1], 2048 * 640 * 2), rw = srd(ka[2], ka[3], 640 * 1920 * 2);
  if (set == 0) {
    if (ADELAY && !br) __builtin_amdgcn_s_sleep(ADELAY);
    sfor<4>([&](auto I) { dma<I * 1024, (NTN > 0)>(rx, xv[I], xs + q0 * 128, sbx); });
    sfor<5>([&](auto I) { dma<STG_W + I * 1024, (NTW > 0)>(rw, woff[I], wsb + q0 * (64 * 3840), sbw); });
  }
  STAMP(6);
  SB();
  const int mt = 2 * m + set;
  const int dom = br * 16 + r;
  const unsigned cb = sbase + CNT;
  volatile LAS unsigned* cnt = (volatile LAS unsigned*)(sm + CNT);
  char* const wsp = (char*)(((u64)kb[3] << 32) | kb[2]);
  const rsrc_t ws = __builtin_amdgcn_make_buffer_rsrc(wsp, 0, WS_END, 0x00020000);
  const v4i ro = srd(ka[4], ka[5], 640 * 640 * 2);
  const unsigned ob = WS_O + (dom * 8 + mt) * 8 * 2560;
  unsigned og[8];
#pragma unroll
  for (int i = 0; i < 8; ++i) {
    og[i] = ob + (i == 0 ? g * 2560 : (i == 7 ? (g ^ 1) * 2560 : ((i - 1) >> 1 >= g >> 1 ? (i + 1) * 2560 : (i - 1) * 2560)));
    asm volatile("" : "+s"(og[i]));
  }
  auto kq = [&](int s) { const unsigned q = q0 + s; return q >= 10 ? q - 10 : q; };
  auto issue_wq1 = [&](auto BB, auto I) {
    constexpr int b = BB, i = I;
    dma<wq_at(b) + i * 1024>(rw, woff[i], g * 160 + (kq(2 * b + (m >> 1)) * 64 + 32 * (m & 1)) * 3840, sbw);
  };
  auto issue_wq = [&](auto BB) { sfor<5>([&](auto I) { issue_wq1(BB, I); }); };
  v2u Oq[5];
  v2u Xq[5];

  auto body = [&](auto BRC) {
    constexpr int BR = BRC, TS = BR ? 8 : 32, LPS = BR ? 40 : 8, NPS = BR ? 9 : 10;
    constexpr int QH = BR ? B_QH : A_QH;
    const v4u pz = {POISON, POISON, POISON, POISON};
    v10u Kc, Kp;
    v4u Vf[5];
    f4 mk;
    v2u selc;
    auto att_prep = [&]() {
      LANE_VARS;
      const int pv = (mt & 1) ? mt - 1 : (m >= 2 ? mt - 3 : 8);
      const lptr kc0 = opq(sm + A_KB + mt * 2560 + l * 8), kp0 = opq(sm + A_KB + pv * 2560 + l * 8);
      const lptr vc0 = opq(sm + A_VB + mt * 2560 + (l >> 2) * 8 + lp * 128), vp0 = opq(sm + A_VB + pv * 2560 + (l >> 2) * 8 + lp * 128);
#pragma unroll
      for (int t = 0; t < 5; ++t) {
        const v2u a = ld8(kc0 + t * 512), b = ld8(kp0 + t * 512);
        Kc[2 * t] = a[0]; Kc[2 * t + 1] = a[1]; Kp[2 * t] = b[0]; Kp[2 * t + 1] = b[1];
      }
#pragma unroll
      for (int t = 0; t < 5; ++t) Vf[t] = cat(trd(vc0 + t * 512), trd(vp0 + t * 512));
      sfor<4>([&](auto RR) {
        constexpr int rr = RR;
        constexpr u64 own = lane_mask([](int l) { const int d = (l & 15) - 4 * (l >> 4) - rr; return d >= 0 && d <= 7; });
        constexpr u64 bef = lane_mask([](int l) { return (l & 15) - 4 * (l >> 4) - rr <= -9; });
        constexpr u64 odd = lane_mask([](int l) { const int p = 4 * (l >> 4) + rr; return p > (l & 15) && p <= 7; });
        const u64 ok = pv != 8 ? own | bef : (r == 0 ? own : ((m & 1) ? own | odd : own | bef));
        mk[rr] = LANES(ok) ? 0.f : -1e30f;
      });
      sfor<2>([&](auto I) {
        constexpr int i = I;
        const unsigned half = LANES(lane_mask([](int l) { return (l & 15) - 4 * (l >> 4) == 2 * i; })) ? 0xffffu : 0u;
        selc[i] = LANES(lane_mask([](int l) { return (l & 15) - 4 * (l >> 4) > 2 * i; })) ? 0xffffffffu : half;
      });
    };
    unsigned ra[5];
    auto set_ra = [&]() {
      LANE_VARS;
      const bool lo = LANES(0xffffffffull);
      ra[0] = sbase + B_R + m * B_RS + l * 8;
      ra[1] = ra[0] + (lo ? 512 : 1024 + (l >> 3) * 64);
      ra[2] = ra[1] + (lo ? 768 : 512);
      ra[3] = sbase + B_R + lg * B_RS + lq * 8 + lp * 32;
      ra[4] = ra[3] + 1536 + (lp >> 1) * 64;
      pin(ra);
    };
    v2u F[7];
    auto phi_k = [&](lptr kp, lptr rp, auto&& mid) {
      v2u X[5];
#pragma unroll
      for (int t = 0; t < 5; ++t) X[t] = ld8(kp + t * 8);
      SB();
      mid();
      SB();
      f4 e[10];
      fm_exp(X, F, e, [](auto) {});
      const float ip = rcp(sum20(e)), in = rcp(sum20(e + 5));
      SB();
#pragma unroll
      for (int c = 0; c < 5; ++c) st16(rp + c * 16, cat(pk4(e[2 * c] * (2 * c < 5 ? ip : in)), pk4(e[2 * c + 1] * (2 * c + 1 < 5 ? ip : in))));
    };

    if (set == 0) {
      v4u xr[10][2][2];
      unsigned wq0;
      lptr kp = sm, rp = sm;
      {
        const int l = tid & 63, lg = l >> 4, li = l & 15, lq = (l >> 2) & 3, lp = l & 3;
        unsigned hv = 0;
        if constexpr (!BR) {
          const int pos = 4 * m + ((l >> 3) & 3);
          const int hrow = r * 128 + 2 * pos - (m < 2 ? 15 : 32);
          hv = r == 0 ? OOB : hrow * 1280 + (((l & 7) ^ (pos >> 1)) << 4);
        }
        auto issue = [&](auto E) {
          constexpr int e = E, s = e / NPS, i = e % NPS;
          if constexpr (s >= 10) {
            issue_wq1(IC<(e - 10 * NPS) / 5>{}, IC<(e - 10 * NPS) % 5>{});
          } else if constexpr (i < 4) {
            dma<slot_of(s) + i * 1024, (s < NTN)>(rx, xv[i], xs + kq(s) * 128, sbx);
          } else if constexpr (i < 9) {
            dma<slot_of(s) + STG_W + (i - 4) * 1024, (s < NTW)>(rw, woff[i - 4], wsb + kq(s) * (64 * 3840), sbw);
          } else {
            dma_lo<slot_of(s) + 16384>(rx, hv, kq(s) * 128, sbh);
          }
        };
        const unsigned xg = sbase + (32 * m + li) * 128 + ((lg ^ (li >> 1)) << 4);
        wq0 = (4 * lg + lq) * 160 + lp * LPS;
        sfor<10 * NPS + 15>([&](auto E) {
          constexpr int e = E;
          if constexpr (e >= 9 && (!BR || e < 10 * NPS + WQH)) issue(E);
          sfor<10>([&](auto S) {
            constexpr int s = S, bp = bar_pos(s, NPS);
            if constexpr (bp == e + 1) {
              wait_vm<bp - (s + 1) * NPS>();
              STAMP3(16 + s);
              lds_bar();
              const lptr a0 = ladd(xg, slot_of(s)), a1 = ladd(xg ^ 64, slot_of(s));
              xr[s][0][0] = ld16(a0); xr[s][0][1] = ld16(a0 + 2048);
              xr[s][1][0] = ld16(a1); xr[s][1][1] = ld16(a1 + 2048);
              if constexpr (BR && s == 9) {
                load_wphi(F, opq(sm + B_WPHI + lg * 800 + lq * 40 + lp * 8), opq(sm + B_WPHI + lg * 800 + lq * 40 + lp * 160));
                kp = opq(sm + B_PHI + lg * 10240 + mt * 1280 + li * 40);
                rp = opq(sm + B_PHI + lg * 10240 + mt * 1280 + li * 80);
              }
              SB();
            }
          });
          if constexpr (e + 1 == bar_pos(9, NPS) + B10L) { STAMP3(26); lds_bar(); STAMP3(27); }
        });
      }
      if constexpr (BR) {
        if (tid < 8) st8(sm + B_QH + tid * 640, (v2u){0x3f80u, 0u});
        STAMP3(28);
        lds_bar();
        STAMP(2);
        phi_k(kp, rp, [&]() { sfor<20 - WQH>([&](auto I) { issue_wq1(IC<(I + WQH) / 5>{}, IC<(I + WQH) % 5>{}); }); });
        STAMP3(29);
        lds_wait<C_V, 4>(cb, 0u);
        issue_wq(IC<4>{});
        wait_vm<5>();
        STAMP(3);
        barx();
      } else {
        issue_wq(IC<3>{});
        wait_vm<15>();
        barx();
      }
      STAMP(4);
      f4 acc[2][5];
      v4u wf[2][5];
      { const lptr b = opq(sm + wq_at(0) + wq0); read_w<TS>(wf[0], b, b + 2560); }
      lptr qh;
      unsigned cfl = 0;
      sfor<5>([&](auto BB) {
        constexpr int b = BB;
        constexpr bool cert = b < 4 && (!BR || b == 3);
        const lptr b0 = opq(sm + wq_at(b) + wq0);
        unsigned cv = 0;
        auto paced = [&](auto I) {
          if constexpr (!BR && b == 0) issue_wq1(IC<4>{}, I);
          SB();
        };
        kstep<true, TS, b == 0>(wf[0], xr[2 * b][0][0], xr[2 * b][0][1], acc, wf[1], b0 + 5120);
        if constexpr (cert) wait_vm<(b < 2 ? 10 : 15 - 5 * b)>();
        if constexpr (!BR || b > 0) arrive<C_PROG + b>(cb, 1u);
        paced(IC<0>{});
        kstep<true, TS, false>(wf[1], xr[2 * b][1][0], xr[2 * b][1][1], acc, wf[0], b0 + 10240);
        if constexpr (cert) cv = cnt[C_PROG + b];
        if constexpr (b == 4) cv = cnt[C_KV];
        if constexpr (BR && b == 4) cfl = cnt[C_FL];
        if constexpr (b == 4) qh = opq(sm + QH + m * 2560 + lane_id() * 8);
        if constexpr (BR && b == 2) set_ra();
        paced(IC<1>{});
        paced(IC<2>{});
        kstep<true, TS, false>(wf[0], xr[2 * b + 1][0][0], xr[2 * b + 1][0][1], acc, wf[1], b0 + 15360);
        paced(IC<3>{});
        if constexpr (b < 4) {
          if constexpr (cert) lds_wait<C_PROG + b, 4>(cb, cv);
          kstep<true, TS, false>(wf[1], xr[2 * b + 1][1][0], xr[2 * b + 1][1][1], acc, wf[0], opq(sm + wq_at(b + 1) + wq0));
        } else {
          lds_wait<C_KV, 4>(cb, cv);
          v2u pq;
          sfor<11>([&](auto N) {
            constexpr int n = N;
            if constexpr (n < 10) mmat<true, false>(acc[1 - n / 5][n % 5], wf[1][n % 5], xr[9][1][1 - n / 5]);
            if constexpr (n == 5) asm volatile("s_nop 2");
            SB();
            if constexpr (n > 5) st8(qh + (n - 6) * 512, pq);
            SB();
            if constexpr (n >= 5 && n < 10) { pq = pk4(acc[1][n - 5]); asm volatile("" : "+v"(pq)); }
            SB();
          });
          arrive<C_Q>(cb + m * 4, 1u);
          if constexpr (!BR) arrive<C_PROG + 5>(cb, 1u);
        }
        paced(IC<4>{});
        STAMP2(26 + b);
      });
      settle(acc[0]);
#pragma unroll
      for (int t = 0; t < 5; ++t) Xq[t] = pk4(acc[0][t]);
      if constexpr (BR) lds_wait<C_FL, 1>(cb, cfl);
    } else {
      const int part = m & 1, rh = m >> 1;
      const int l = tid & 63, lg = l >> 4, li = l & 15, lq = (l >> 2) & 3, lp = l & 3;
      if (m == 1 && l < 32) cnt[l] = 0;
      f4 acc[5][5];
#pragma unroll
      for (int j = 0; j < (BR ? 4 : 5); ++j) {
#pragma unroll
        for (int t = 0; t < 5; ++t) acc[j][t] = (f4){0.f, 0.f, 0.f, 0.f};
        pin(acc[j]);
      }
      unsigned xa = sbase + (64 * rh + li) * 128 + (((lg ^ (li >> 1)) << 4) ^ (rh * 64)), xb = xa ^ 64;
      unsigned wa = sbase + STG_W + part * 10240 + (4 * lg + lq) * 160 + lp * LPS + rh * 5120;
      unsigned wb = wa + 5120 - rh * 10240, xh = xa + (2 - rh) * 8192;
      unsigned ad[11];
      if constexpr (BR) {
        ad[0] = sbase + li * 40 + (part ? B_VIMG + lg * 5120 + rh * 2560 : B_PHI + lg * 10240 + rh * 5120);
        ad[1] = sbase + B_WPHI + lg * 800 + lq * 40 + lp * 8;
        ad[2] = sbase + B_WPHI + lg * 800 + lq * 40 + lp * 160;
        ad[3] = sbase + B_PHI + lg * 10240 + mt * 1280 + li * 40;
        ad[4] = sbase + B_PHI + lg * 10240 + mt * 1280 + li * 80;
        ad[5] = sbase + B_PHI + m * 10240 + (4 * lg + lq) * 80 + lp * 8;
        ad[6] = sbase + B_VIMG + m * 5120 + (4 * lg + lq) * 40 + lp * 8;
        ad[7] = lp == 1 ? sbase + B_QH : ad[6] + 32;
        ad[8] = lg * 4096 + li * 16;
        ad[9] = l < 32 ? ad[8] : OOB;
        ad[10] = li < 8 ? (lg >> 1) * 4096 + ((lg & 1) * 8 + li) * 16 : OOB;
        pin(ad);
        set_ra();
      }
      set_woff(woff, l, false, 1280);
      pin(woff);

      auto clear_slots = [&]() {
        if constexpr (BR) {
          const unsigned so = WS_KP + (g * 32 + m * 8) * 4096 + r * 256;
          gst16(ws, ad[8], so, pz);
          gst16(ws, ad[9], so + 4 * 4096, pz);
          gst16(ws, ad[10], so + 6 * 4096, pz);
          gst16(ws, l < 16 && m < 2 ? l * 16 : OOB, WS_KR + (g * 32 + ((m * 16 + r) & 31)) * 256, pz);
          SB();
        }
      };
      unsigned ft = m != 3 ? OOB : l < 2 ? WS_F3 + (dom * 2 + l) * 1024 + g * 128 : BR && l == 2 ? WS_F1 + (g * 16 + r) * 128 : OOB;
      asm volatile("" : "+v"(ft));
      __builtin_amdgcn_s_setprio(GPRIO1);
      lds_bar();
      STAMP(1);
      {
        constexpr int NX = BR ? 4 : 5;
        Frag f0, f1;
        {
          v2u lo;
          sfor<NX + 10>([&](auto I) { frag_read<NX, TS, I>(f0, lo, (lptr)(size_t)xa, (lptr)(size_t)xh, (lptr)(size_t)wa); });
          SB();
          bump(xa, 3 * STG); bump(wa, 3 * STG);
          if constexpr (!BR) bump(xh, 3 * STG);
        }
#pragma unroll 1
        for (int s = 0; s < 9; ++s) {
          kstep4<NX, 4, TS>(f0, f1, acc, xb, xh, wb, (s & 3) == 0 ? 3 * STG : -STG);
          if (s == 2) {
            if (BR && l < 50) dma<B_WPHI>(srd(ka[6], ka[7], 32 * 400 * 2), l * 16, (g * 4 + m) * 800, sbase + m * 800);
            if (m == 3)
              asm volatile("s_mov_b32 m0, %2\n\ts_nop 0\n\tbuffer_load_dword %0, %1, 0 offen sc1 lds" :: "v"(ft), "s"(ws), "s"(sbase + JUNK) : "memory");
            if (m == 1 && l < 4)
              asm volatile("s_mov_b32 m0, %2\n\ts_nop 0\n\tbuffer_load_dword %0, %1, 0 offen sc1 lds"
                           :: "v"((l < 2 ? WS_E3 + (dom << 7) : WS_E2 + (g << 7) - 8) + l * 4), "s"(ws), "s"(cb + 128) : "memory");
          }
          if (s == 6) {
            wait_vm<0>();
          }
          lds_bar();
          kstep4<4, NX, TS>(f1, f0, acc, xa, xh, wa, (s & 3) == 3 ? 3 * STG : -STG);
        }
        STAMP3(20);
        kstep4<NX, 4, TS>(f0, f1, acc, xb, xh, wb, 0);
        STAMP3(21);
        lds_bar();
        STAMP3(22);
        if constexpr (BR) {
          v2u pq;
          sfor<26>([&](auto N) {
            constexpr int n = N, j = n / 5, t = n % 5;
            if constexpr (j < 4) mmat<true, false>(acc[j][t], f1.w[t], f1.x[j]);
            if constexpr (n == 0) load_wphi(F, (lptr)(size_t)ad[1], (lptr)(size_t)ad[2]);
            SB();
            if constexpr (n > 5) st8((lptr)(size_t)ad[0] + (n - 6) % 5 * 8, pq);
            if constexpr (n > 5 && (n - 6) % 5 == 4 && n < 25) bump(ad[0], part ? 640 : 1280);
            SB();
            if constexpr (n >= 5 && n < 25) { pq = pk4(acc[j - 1][t]); asm volatile("" : "+v"(pq)); }
            SB();
          });
        } else {
          kstep4<4, 0, TS>(f1, f0, acc, xa, xh, wa, 0);
        }
      }
      STAMP(5);
      auto touch_wo = [&]() {
        if (WOT) touch(ro, ((m * 32 + idx) * 64 + l) * 128, 0, sbase + JUNK);
      };
      if constexpr (BR) {
        const lptr fp = (lptr)(size_t)ad[5], dp = (lptr)(size_t)ad[6], ep = (lptr)(size_t)ad[7];
        const unsigned so = WS_KP + (g * 32 + m * 8) * 4096 + r * 256;
        const int rid = m * 16 + r;
        lds_bar();
        STAMP(2);
        v4u D[4][2];
        v2u tag2 = ld8(sm + CNT + 136);
        phi_k((lptr)(size_t)ad[3], (lptr)(size_t)ad[4], [&]() {
#pragma unroll
          for (int ks = 0; ks < 4; ++ks) {
            D[ks][0] = cat(trd(dp + ks * 1280), trd(dp + ks * 1280 + 640));
            D[ks][1] = cat(trd(ep + ks * 1280), trd(ep + ks * 1280 + 640));
          }
          arrive<C_V>(cb, 1u);
          clear_slots();
          touch_wo();
        });
        tag2 = next_tag(tag2);
        wait_vm<WOT>();
        STAMP(3);
        __builtin_amdgcn_s_setprio(GPRIO);
        lds_bar();
        if (m == 3) gst8(ws, l < FW ? l * 8u : OOB, WS_F1 + (g * 16 + r) * 128, tag2);
        STAMP(4);
        v2u cf1;
        {
          f4 d0[3], d1[3];
#pragma unroll
          for (int b = 0; b < 3; ++b) d0[b] = d1[b] = (f4){0.f, 0.f, 0.f, 0.f};
          v4u Fk[4][3];
#pragma unroll
          for (int ks = 0; ks < 4; ++ks)
#pragma unroll
            for (int b = 0; b < 3; ++b) Fk[ks][b] = cat(trd(fp + ks * 2560 + b * 32), trd(fp + ks * 2560 + 1280 + b * 32));
          arrive<C_KV>(cb, 1u);
#pragma unroll
          for (int ks = 0; ks < 4; ++ks)
#pragma unroll
            for (int b = 0; b < 3; ++b) {
              d0[b] = mma(D[ks][0], Fk[ks][b], d0[b]);
              d1[b] = mma(D[ks][1], Fk[ks][b], d1[b]);
            }
          SB();
          if (CFE) cf1 = gld8(ws, m < 2 && l < 16 ? l * 128u : OOB, WS_F1 + g * 2048);
          SB();
#pragma unroll
          for (int b = 0; b < 3; ++b) d1[b][1] = lg == 1 ? 0.0625f : d1[b][1];
          gst16(ws, ad[8], so, unp(cat(pk4(d0[0]), pk4(d0[1]))));
          gst16(ws, ad[9], so + 4 * 4096, unp(cat(pk4(d1[0]), pk4(d1[1]))));
          gst16(ws, ad[10], so + 6 * 4096, unp(cat(pk4(d0[2]), pk4(d1[2]))));
        }
        STAMP(16);
        if (m >= 2) {
          lds_wait<C_KV, 4>(cb, 0u);
          __builtin_amdgcn_s_setprio(0);
          sfor<5>([&](auto J) {
            constexpr int j = 4 - J;
            if constexpr (j < 4) lds_wait<C_PROG + 4 - j, 4, WSLP>(cb, 0u);
            sfor<10>([&](auto I) {
              dma<B_WO + j * BLK + I * 1024>(ro, woff[I % 5], g * 160 + (j * 128 + 64 * part + 32 * (I / 5)) * 1280, sbase + part * 10240);
            });
          });
        }
        if (m < 2) {
          v4u pv[4];
          v4u sel;
          f4 sum;
#pragma unroll
          for (int e = 0; e < 4; ++e) sel[e] = (li >> 1) == e ? 0x3f80u << ((li & 1) * 16) : 0u;
          unsigned kro = l < 32 ? li * 16 + lg * 8 : OOB;
          asm volatile("" : "+v"(sel), "+v"(kro));
          if (CFE) check_flags(ws, WS_F1 + g * 2048, l < 16 ? l * 128u : OOB, tag2, cf1);
          const unsigned fo = l < 16 ? l * 128u : OOB;
          if (RSLP) __builtin_amdgcn_s_sleep(RSLP);
          if (!CFE) poll_flags(ws, WS_F1 + g * 2048, fo, tag2);
          if (m == 0) {
            arrive<C_FL>(cb, 1u);
            if (r == 0) gst8(ws, l == 0 ? 0u : OOB, WS_E2 + (g << 7), tag2);
          }
          STAMP(18);
#pragma unroll 1
          for (unsigned it = 0; it < SPIN_CAP; ++it) {
            asm volatile("" ::: "memory");
#pragma unroll
            for (int i = 0; i < 4; ++i) pv[i] = gld16(ws, l * 16, WS_KP + (g * 32 + rid) * 4096 + i * 1024);
            {
              sum = (f4){0.f, 0.f, 0.f, 0.f};
#pragma unroll
              for (int i = 0; i < 4; ++i) sum = mma(sel, pv[i], sum);
              asm volatile("" : "+v"(sum));
            }
            if (!any_poison(mx4(mx4(mx4(mx4(0u, pv[0]), pv[1]), pv[2]), pv[3]))) break;
          }
          STAMP(7);
          gst8(ws, kro, WS_KR + (g * 32 + rid) * 256, unp(pk4(sum)));
          __builtin_amdgcn_s_setprio(0);
          STAMP(8);
        }
      } else {
        barx();
        touch_wo();
        settle(acc[3]);
        pin(acc[0]); pin(acc[1]); pin(acc[2]); pin(acc[4]);
        const lptr hp = opq(sm + A_HP + part * 5120 + l * 16);
        if (rh) {
#pragma unroll
          for (int t = 0; t < 5; ++t) st16(hp + t * 1024, __builtin_bit_cast(v4u, acc[4][t]));
          arrive<C_HP>(cb, 1u);
        }
        const lptr ob = opq(sm + A_KB + part * (A_VB - A_KB) + l * 8);
        const lptr ob2 = opq(ob + rh * 10240);
#pragma unroll
        for (int j = 0; j < 4; ++j)
#pragma unroll
          for (int t = 0; t < 5; ++t) st8(ob2 + (j * 5 + t) * 512, pk4(acc[j][t]));
        if (!rh) {
          lds_wait<C_HP, 2>(cb, 0u);
#pragma unroll
          for (int t = 0; t < 5; ++t) st8(ob + (40 + t) * 512, pk4(acc[4][t] + __builtin_bit_cast(f4, ld16(hp + t * 1024))));
        }
        arrive<C_KV>(cb, 1u);
        __builtin_amdgcn_s_setprio(0);
        STAMP(3);
        sfor<5>([&](auto J) {
          constexpr int j = 4 - J;
          if constexpr (j == 0) {
            lds_wait<C_KV, 4>(cb, 0u);
            att_prep();
            pin(Vf);
            asm volatile("" : "+v"(Kc), "+v"(Kp), "+v"(mk), "+v"(selc));
          }
          lds_wait<C_PROG + 5 - j, 4, (j ? WSLP : 1)>(cb, 0u);
          sfor<5>([&](auto I) { dma<A_WO + j * BLK + I * 1024>(ro, woff[I], g * 160 + (j * 128 + 32 * m) * 1280, sbw); });
        });
      }
      if constexpr (BR) lds_wait<C_Q, 1>(cb + m * 4, 0u);
      __builtin_amdgcn_s_setprio(0);
      const lptr qh = opq(sm + QH + m * 2560 + l * 8);
#pragma unroll
      for (int t = 0; t < 5; ++t) Xq[t] = ld8(qh + t * 512);
    }
    STAMP(9);
    LANE_VARS;
    auto store_o = [&](auto I) {
      constexpr int i = I;
      if constexpr (i < 2) gst16(ws, l * 16, og[0] + i * 1024, cat(Oq[2 * i], Oq[2 * i + 1]));
      else {
        auto a = __builtin_amdgcn_permlane32_swap(Oq[4][0], Oq[4][0], false, false);
        auto b = __builtin_amdgcn_permlane32_swap(Oq[4][1], Oq[4][1], false, false);
        gst16(ws, l < 32 ? l * 16 : OOB, og[0] + 2048, (v4u){a[0], b[0], a[1], b[1]});
      }
    };
    constexpr int TO = (BR ? 4 : 16) * 160;
    constexpr int QS = (BR ? 20 : 4) * 160;
    const unsigned og3 = og[0] < og[7] ? og[0] : og[7];
    unsigned wl[8], wt[4];
    auto set_w = [&]() {
    const unsigned w0 = sbase + (BR ? B_WO : A_WO) + lq * 160 + lp * 40 - 5 * ob;
#pragma unroll
    for (int i = 0; i < 8; ++i) wl[i] = (w0 + lg * QS) + 5 * og[i];
#pragma unroll
    for (int p = 0; p < 4; ++p) wt[p] = (w0 + (lg & 1) * QS + (lg >> 1) * 12800) + 5 * (p < 3 ? og[2 * p + 1] : og3);
    pin(wl);
    pin(wt);
    };
    v2u wf[RING][10];
    auto rd = [&](auto K) {
      constexpr int s = K / 10, t = K % 10 >> 1, h = K & 1;
      if constexpr (s < 20) {
        if constexpr (step_tail(s)) wf[s % RING][K % 10] = trd((lptr)(size_t)wt[(s - 2) / 5] + 4 * TO + h * 2 * QS + t * 8);
        else wf[s % RING][K % 10] = trd((lptr)(size_t)wl[step_e(s) >> 1] + ((step_e(s) & 1) * 2 + h) * TO + t * 8);
      }
    };
    constexpr int P0 = BR ? PRE0 : 0, P1 = P0 ? PRE1B : PRE1;
    if constexpr (!BR) {
      if (set == 0) {
        att_prep();
        arrive<C_WO>(cb, 1u);
        if (APR == 2) __builtin_amdgcn_s_setprio(2);
      } else if (APR) {
        __builtin_amdgcn_s_setprio(1);
      }
      SB();
      constexpr float CS = 0.22360679774997896f * 1.4426950408889634f;
      f4 sc[4], sp[4];
      sfor<4>([&](auto H) {
        constexpr int h = H;
        const bool lo = LANES(lane_mask([](int l) { return (l >> 4) >= h; })), hi = LANES(lane_mask([](int l) { return (l >> 4) <= h; }));
        const v4u qh = {lo ? Xq[h][0] : 0u, lo ? Xq[h][1] : 0u, hi ? Xq[h + 1][0] : 0u, hi ? Xq[h + 1][1] : 0u};
        sc[h] = mma(__builtin_shufflevector(Kc, Kc, 2 * h, 2 * h + 1, 2 * h + 2, 2 * h + 3), qh, mk);
        sp[h] = mma(__builtin_shufflevector(Kp, Kp, 2 * h, 2 * h + 1, 2 * h + 2, 2 * h + 3), qh, mk);
        SB();
      });
      float mx[4];
      sfor<4>([&](auto H) {
        constexpr int h = H;
        sfor<4>([&](auto RR) {
          constexpr int rr = RR;
          sc[h][rr] = LANES(lane_mask([](int l) { return 4 * (l >> 4) + rr <= (l & 15); })) ? sc[h][rr] : sp[h][rr];
        });
        SB();
      });
#pragma unroll
      for (int h = 0; h < 4; ++h) mx[h] = vmax3(sc[h][0], sc[h][1], sc[h][2]);
      SB();
#pragma unroll
      for (int h = 0; h < 4; ++h) mx[h] = vmax3(mx[h], sc[h][3], sc[h][3]);
      asm volatile("s_nop 1" : "+v"(mx[0]), "+v"(mx[1]), "+v"(mx[2]), "+v"(mx[3]));
      {
        auto a = __builtin_amdgcn_permlane32_swap(f2u(mx[0]), f2u(mx[2]), false, false);
        auto b = __builtin_amdgcn_permlane32_swap(f2u(mx[1]), f2u(mx[3]), false, false);
        float u = vmax3(u2f(a[0]), u2f(a[1]), u2f(a[1])), v = vmax3(u2f(b[0]), u2f(b[1]), u2f(b[1]));
        asm volatile("s_nop 1" : "+v"(u), "+v"(v));
        auto c = __builtin_amdgcn_permlane16_swap(f2u(u), f2u(v), false, false);
        const float t = -CS * vmax3(u2f(c[0]), u2f(c[1]), u2f(c[1]));
        auto d = __builtin_amdgcn_permlane16_swap(f2u(t), f2u(t), false, false);
        auto e0 = __builtin_amdgcn_permlane32_swap(d[0], d[0], false, false);
        auto e1 = __builtin_amdgcn_permlane32_swap(d[1], d[1], false, false);
        mx[0] = u2f(e0[0]); mx[2] = u2f(e0[1]); mx[1] = u2f(e1[0]); mx[3] = u2f(e1[1]);
      }
      SB();
#pragma unroll
      for (int h = 0; h < 4; ++h)
#pragma unroll
        for (int rr = 0; rr < 4; ++rr) sc[h][rr] = __builtin_fmaf(sc[h][rr], CS, mx[h]);
      SB();
#pragma unroll
      for (int h = 0; h < 4; ++h)
#pragma unroll
        for (int rr = 0; rr < 4; ++rr) sc[h][rr] = ex2(sc[h][rr]);
      SB();
      if (set) { wait_vm<0>(); arrive<C_WO>(cb, 1u); }
      SB();
      v4u ones = {0x3f803f80u, 0x3f803f80u, 0x3f803f80u, 0x3f803f80u};
      f4 sm_[4], oh[4], ol[4];
      v4u P[4];
#pragma unroll
      for (int h = 0; h < 4; ++h) {
        const v2u pp = pk4(sc[h]);
        P[h] = (v4u){pp[0] & selc[0], pp[1] & selc[1], pp[0] & ~selc[0], pp[1] & ~selc[1]};
      }
      asm volatile("s_nop 1" : "+v"(P[0]), "+v"(P[1]), "+v"(P[2]), "+v"(P[3]), "+v"(ones));
#pragma unroll
      for (int h = 0; h < 4; ++h) mmat<true, true>(sm_[h], ones, P[h]);
#pragma unroll
      for (int h = 0; h < 4; ++h) { mmat<true, true>(oh[h], Vf[h], P[h]); mmat<true, true>(ol[h], Vf[h + 1], P[h]); }
      SB();
      float is[4];
#pragma unroll
      for (int h = 0; h < 4; ++h) is[h] = rcp(sm_[h][0]);
      sfor<5>([&](auto N) {
        constexpr int n = N;
        SB();
        if constexpr (n == 0) Oq[n] = pk4(oh[0] * is[0]);
        else if constexpr (n == 4) Oq[n] = pk4(ol[3] * is[3]);
        else {
          const bool up = LANES(lane_mask([](int l) { return (l >> 4) >= n; }));
          Oq[n] = pk4((up ? oh[n] : ol[n - 1]) * (up ? is[n] : is[n - 1]));
        }
      });
    } else {
      v4u kr[2];
      auto load_kr = [&]() {
#pragma unroll
        for (int i = 0; i < 2; ++i) kr[i] = gld16(ws, l * 16, WS_KR + (g * 4 + m) * 2048 + i * 1024);
      };
      v2u Pq[10];
      {
        f4 e[10];
        fm_exp(Xq, F, e, [&](auto I) {
          if (I == KRPOS && !set) load_kr();
          if (I == 0 && set) { wait_vm<0>(); arrive<C_WO>(cb, 1u); }
        });
#pragma unroll
        for (int u = 0; u < 10; ++u) Pq[u] = pk4(e[u]);
      }
      pin(Pq);
      if constexpr (P0 > 0) {
        set_w();
        if (!E0L || !set) {
          lds_wait<C_WO, 4>(cb, 0u);
          sfor<P0>(rd);
        }
        SB();
      }
      if (set) { STAMP(17); } else { STAMP(7); }
      if (!set) {
#pragma unroll 1
        for (unsigned it = 0; it < SPIN_CAP; ++it) {
          if (!any_poison(mx4(mx4(0u, kr[0]), kr[1]))) break;
          asm volatile("" ::: "memory");
          load_kr();
        }
        st8((lptr)(size_t)ra[0], (v2u){kr[0][0], kr[0][1]});
        st8((lptr)(size_t)ra[0] + 768, (v2u){kr[0][2], kr[0][3]});
        st8((lptr)(size_t)ra[1], (v2u){kr[1][0], kr[1][1]});
        st8((lptr)(size_t)ra[2], (v2u){kr[1][2], kr[1][3]});
        arrive<C_R>(cb, 1u);
        if (LPR) __builtin_amdgcn_s_setprio(2);
      } else if (LPR >= 4) {
        __builtin_amdgcn_s_setprio(1);
      }
      const lptr r_rd = (lptr)(size_t)ra[3], r_r2 = (lptr)(size_t)ra[4];
      lds_wait<C_R, 4, 0>(cb, 0u);
      STAMP(10);
      f4 np[6], nn[6];
      v2u Rf[15];
      sfor<15>([&](auto I) {
        constexpr int p = I / 5, j = I % 5, i = j < 4 ? 2 * p + (j >> 1) + 6 * (j & 1) : 14 - p;
        Rf[i] = i < 12 ? trd(r_rd + (i / 6) * 768 + (5 - i % 6) * 128) : trd(r_r2 + (i - 12) * 256);
        SB();
      });
      float ip, in, dn, fp_, fn_;
      auto fin = [&](auto T, auto K) {
        constexpr int t = T, k = K;
        if constexpr (k < 4) np[t][k] *= fp_;
        else if constexpr (k < 8) np[t][k - 4] = __builtin_fmaf(nn[t][k - 4], fn_, np[t][k - 4]);
        else Oq[t][k - 8] = pk2(np[t][2 * k - 16], np[t][2 * k - 15]);
      };
      sfor<60>([&](auto N) {
        constexpr int n = N, tt = 5 - 2 * (n / 20) - ((n >> 1) & 1), u = (n % 20 >> 2) + 5 * (n & 1);
        const f4 zz = {0.f, 0.f, 0.f, 0.f};
        f4& d = u < 5 ? np[tt] : nn[tt];
        if constexpr (u < 8) d = mma4<(u & 3)>(Rf[(u >> 2) * 6 + 5 - tt], Pq[u], u % 5 ? d : zz);
        else d = mma4<(u & 1) + 2 * (tt & 1)>(Rf[12 + (tt >> 1)], Pq[u], d);
        SB();
        if constexpr (n == 19) ip = rcp(np[5][1]);
        if constexpr (n == 20) in = rcp(nn[5][1]);
        if constexpr (n == 21) dn = __builtin_fmaf(np[5][0], ip, 1e-6f);
        if constexpr (n == 22) dn = __builtin_fmaf(nn[5][0], in, dn);
        if constexpr (n == 23) dn = rcp(dn);
        if constexpr (n == 25) fp_ = ip * dn;
        if constexpr (n == 26) fn_ = in * dn;
        if constexpr (n >= 27 && n < 37) fin(IC<4>{}, IC<n - 27>{});
        if constexpr (n == 38) store_o(IC<2>{});
        if constexpr (n >= 40) fin(IC<(n < 50 ? 3 : 2)>{}, IC<n % 10>{});
        SB();
      });
      store_o(IC<1>{});
      SB();
      sfor<20>([&](auto K) { fin(IC<(K & 1)>{}, IC<(K >> 1)>{}); });
    }

    pin(Oq);
    STAMP(11);
    store_o(IC<0>{});
    if constexpr (!BR) { store_o(IC<1>{}); store_o(IC<2>{}); }
    if (LPR == 1 || (LPR >= 4 && (BR || APR == 2) && !set)) __builtin_amdgcn_s_setprio(0);
    if (P0 && E0L && set) {
      lds_wait<C_WO, 4>(cb, 0u);
      sfor<P0>(rd);
    }
    if constexpr (!P0) lds_wait<C_WO, (BR ? 4 : 8)>(cb, 0u);
    STAMP(12);
    f4 acc[5];
    {
      if constexpr (!P0) set_w();
      constexpr int NW = SPLIT ? 4 : 8;
      const unsigned cs = SPLIT ? cb + set * 4 : cb, fb = WS_F3 + (SPLIT ? dom * 2 + set : dom) * 1024;
      const int role = SPLIT ? m : w;
      const v2u tag3 = next_tag(ld8(sm + CNT + 128));
      sfor<P1 - P0>([&](auto K) { rd(IC<K + P0>{}); });
      SB();
      wait_vm<0>();
      arrive<C_ACK>(cs, 1u);
      if (role == 0) {
        lds_wait<C_ACK, NW>(cs, 0u);
        gst8(ws, l < FW ? l * 8u : OOB, fb + g * 128, tag3);
        STAMP2(BR ? 19 : 7);
      }
      if (LPR == 2 || LPR == 4) __builtin_amdgcn_s_setprio(0);
      sfor<10 * RING - P1>([&](auto K) { rd(IC<K + P1>{}); });
      SB();
      sfor<10>([&](auto N) {
        constexpr int s = N / 5, t = N % 5;
        mmat<true, s == 0>(acc[t], cat(wf[s][2 * t], wf[s][2 * t + 1]), cat(Oq[2 * s], Oq[2 * s + 1]));
        rd(IC<10 * RING + 2 * N>{});
        rd(IC<10 * RING + 2 * N + 1>{});
        SB();
      });
      unsigned vt = (l & 31) * 16 + 2048 + (l >> 5) * 2560;
      unsigned oa[3], la[3];
#pragma unroll
      for (int i = 0; i < 3; ++i) {
        const unsigned p = i * 64 + l, rho = (p * 6554u) >> 16, c = p - rho * 10;
        oa[i] = (i < 2 || l < 32) ? rho * (BR ? 2560 : 5120) + c * 16 : OOB;
        la[i] = sbase + (BR ? B_OUT : A_OUT) + w * 2688 + p * 16 + rho * 8;
      }
      pin(oa); pin(la);
      asm volatile("" : "+v"(vt));
      STAMP2(BR ? 22 : 8);
      if (role == 1) {
        if (OSLP) __builtin_amdgcn_s_sleep(OSLP);
        poll_flags(ws, fb, l < 8 ? l * 128u : OOB, tag3);
        arrive<C_GO>(cs, 1u);
        if ((set || !SPLIT) && g == 0) gst8(ws, l == 0 ? 0u : OOB, WS_E3 + (dom << 7), tag3);
      }
      lds_wait<C_GO, 1>(cs, 0u);
      if (set) __builtin_amdgcn_s_setprio(TPRIO);
      STAMP(13);
      v4u of[20];
      auto ld_o = [&](auto S) {
        constexpr int s = S;
        if constexpr (s < 20) {
          if constexpr (!step_tail(s)) of[s] = gld16(ws, l * 16 + (step_e(s) & 1) * 1024, og[step_e(s) >> 1]);
          else of[s] = gld16(ws, vt, s == 19 ? og3 : og[2 * ((s - 2) / 5) + 1]);
        }
        SB();
      };
      sfor<AHEAD>([&](auto E) { ld_o(IC<E + 2>{}); });
      sfor<90>([&](auto N) {
        constexpr int n = N, s = n / 5 + 2, t = n % 5, k0 = 10 * RING + 20 + n * RDN / 10, k1 = 10 * RING + 20 + (n + 1) * RDN / 10;
        mmat<true, false>(acc[t], cat(wf[s % RING][2 * t], wf[s % RING][2 * t + 1]), of[s]);
        sfor<k1 - k0>([&](auto J) { rd(IC<k0 + J>{}); });
        if constexpr (t == 0) ld_o(IC<AHEAD + s>{});
        if constexpr (n == 0) { STAMP2(20); }
        if constexpr (n == 45) { STAMP2(21); }
        SB();
      });
      settle(acc);
      STAMP(14);
      const lptr tw = opq(sm + (BR ? B_OUT : A_OUT) + w * 2688 + li * 168 + lg * 40);
#pragma unroll
      for (int t = 0; t < 5; ++t) st8(tw + t * 8, pk4(acc[t]));
      const rsrc_t rout = __builtin_amdgcn_make_buffer_rsrc((char*)(((u64)kb[1] << 32) | kb[0]), 0, 2048 * 1280 * 2, 0x00020000);
      const int row0 = BR ? (r * 128 + 16 * mt) : (r * 128 + 64 * (m >> 1) + 32 * (mt & 1) + (m & 1));
      const unsigned so = row0 * 2560 + BR * 1280 + g * 160;
      v4u od[3];
#pragma unroll
      for (int i = 0; i < 3; ++i) od[i] = cat(ld8((lptr)(size_t)la[i]), ld8((lptr)(size_t)la[i] + 8));
#pragma unroll
      for (int i = 0; i < 3; ++i) gst16<OSC>(rout, oa[i], so, od[i]);
      asm volatile(".Lfk_end%0:" :: "n"(BR));
    }
  };
  if (br) body(IC<1>{});
  else body(IC<0>{});
}

#if defined(__HIP_DEVICE_COMPILE__) && NOPK
#pragma clang attribute pop
#endif

extern "C" void kernel_launch(void* const* d_in, const int* in_sizes, int n_in, void* d_out, int out_size, void* d_ws,
                              size_t ws_size, hipStream_t stream) {
  Params p;
  p.xA = (const u16*)d_in[0]; p.wA = (const u16*)d_in[1]; p.oA = (const u16*)d_in[2]; p.pA = (const u16*)d_in[5];
  p.xB = (const u16*)d_in[3]; p.wB = (const u16*)d_in[4]; p.pB = (const u16*)d_in[5]; p.oB = (const u16*)d_in[6];
  p.out = (u16*)d_out; p.ws = (char*)d_ws;
  fused_kernel<<<256, 512, 0, stream>>>(p);
}
```
